```python
import torch

def _softmax(x, axis=-1):
    return torch.softmax(x, dim=axis)
def _gelu(x):
    return torch.nn.functional.gelu(x, approximate="tanh")
def _silu(x):
    return torch.nn.functional.silu(x)
def _sigmoid(x):
    return torch.sigmoid(x)
def _layernorm(x, axis=-1, eps=1e-5):
    m = x.mean(dim=axis, keepdim=True)
    v = x.var(dim=axis, keepdim=True, unbiased=False)
    return (x - m) / torch.sqrt(v + eps)
def _scatter_add(tgt, idx, src, axis=0):
    return tgt.clone().index_add_(axis, idx, src)
def _segment_sum(src, seg, n):
    out = torch.zeros((n,) + src.shape[1:], dtype=src.dtype)
    return out.index_add_(0, seg, src)
def _pad(x, pw):
    flat = tuple(v for pair in reversed(pw) for v in pair)
    return torch.nn.functional.pad(x, flat)

kernel_name = 'concat_heads__mha_block_cached__gqa_block_cached__near_ridge'

def setup_inputs():

    _w = (lambda a: (lambda t: t.to(torch.bfloat16) if t.is_floating_point() else t)(torch.as_tensor(a)))
    def _A_setup():
        B, Sq, D = 16, 96, 640
        H, Sk, Dh = 32, 512, 192
        return {
            "x": _w((lambda *s: torch.randn(*s, dtype=torch.bfloat16) * 0.8)(B, Sq, D)),
            "k_cache": _w((lambda *s: torch.randn(*s, dtype=torch.bfloat16) * 0.8)(B, H, Sk, Dh)),
            "v_cache": _w((lambda *s: torch.randn(*s, dtype=torch.bfloat16) * 0.05)(B, H, Sk, Dh)),
            "wq": _w((lambda *s: torch.randn(*s, dtype=torch.bfloat16) * 0.8)(D, H * Dh) * (D ** -0.5)),
            "wo": _w((lambda *s: torch.randn(*s, dtype=torch.bfloat16) * 0.05)(H * Dh, D) * ((H * Dh) ** -0.5)),
        }
    _A_in = _A_setup()

    def _B_setup():
        B, Sq, D = 16, 96, 640
        Hq, Hkv, Sk, Dh = 32, 32, 512, 192
        assert Hq % Hkv == 0, (Hq, Hkv)
        return {
            "x": _w((lambda *s: torch.randn(*s, dtype=torch.bfloat16) * 0.8)(B, Sq, D)),
            "k_cache": _w((lambda *s: torch.randn(*s, dtype=torch.bfloat16) * 0.8)(B, Hkv, Sk, Dh)),
            "v_cache": _w((lambda *s: torch.randn(*s, dtype=torch.bfloat16) * 0.05)(B, Hkv, Sk, Dh)),
            "wq": _w((lambda *s: torch.randn(*s, dtype=torch.bfloat16) * 0.8)(D, Hq * Dh) * (D ** -0.5)),
            "wo": _w((lambda *s: torch.randn(*s, dtype=torch.bfloat16) * 0.05)(Hq * Dh, D) * ((Hq * Dh) ** -0.5)),
        }
    _B_in = _B_setup()

    return {
        "A_x": _A_in["x"],
        "A_k_cache": _A_in["k_cache"],
        "A_v_cache": _A_in["v_cache"],
        "A_wq": _A_in["wq"],
        "A_wo": _A_in["wo"],
        "B_x": _B_in["x"],
        "B_k_cache": _B_in["k_cache"],
        "B_v_cache": _B_in["v_cache"],
        "B_wq": _B_in["wq"],
        "B_wo": _B_in["wo"],
    }

def reference(A_x, A_k_cache, A_v_cache, A_wq, A_wo, B_x, B_k_cache, B_v_cache, B_wq, B_wo):
    def _A_ref(x, k_cache, v_cache, wq, wo):
        B, Sq, D = x.shape
        H, Dh = 32, 192
        q = (lambda x, *d: x.permute(*d))((x @ wq).reshape(B, Sq, H, Dh), 0, 2, 1, 3)
        scores = torch.einsum("bhqd,bhkd->bhqk", q, k_cache) * (Dh ** -0.5)
        p = _softmax(scores, axis=-1)
        o = torch.einsum("bhqk,bhkd->bhqd", p, v_cache)
        return (lambda x, *d: x.permute(*d))(o, 0, 2, 1, 3).reshape(B, Sq, H * Dh) @ wo

    def _B_ref(x, k_cache, v_cache, wq, wo):
        B, Sq, D = x.shape
        Hq, Dh = 32, 192
        Hkv = k_cache.shape[1]
        rep = Hq // Hkv
        q = (lambda x, *d: x.permute(*d))((x @ wq).reshape(B, Sq, Hq, Dh), 0, 2, 1, 3)
        k = (lambda x, n, axis: torch.repeat_interleave(x, n, dim=axis))(k_cache, rep, axis=1)
        v = (lambda x, n, axis: torch.repeat_interleave(x, n, dim=axis))(v_cache, rep, axis=1)
        scores = torch.einsum("bhqd,bhkd->bhqk", q, k) * (Dh ** -0.5)
        p = _softmax(scores, axis=-1)
        o = torch.einsum("bhqk,bhkd->bhqd", p, v)
        return (lambda x, *d: x.permute(*d))(o, 0, 2, 1, 3).reshape(B, Sq, Hq * Dh) @ wo

    a = _A_ref(A_x, A_k_cache, A_v_cache, A_wq, A_wo)
    b = _B_ref(B_x, B_k_cache, B_v_cache, B_wq, B_wo)
    return torch.concatenate([a, b], axis=-1)
```

```cpp
#include <hip/hip_runtime.h>
#include <hip/hip_bf16.h>

typedef __attribute__((ext_vector_type(8))) __bf16 bf16x8;
typedef __attribute__((ext_vector_type(2))) __bf16 bf16x2;
typedef __attribute__((ext_vector_type(4))) short s16x4;
typedef __attribute__((ext_vector_type(8))) short s16x8;
typedef __attribute__((ext_vector_type(2))) float f32x2;
typedef __attribute__((ext_vector_type(4))) float f32x4;
typedef __attribute__((ext_vector_type(16))) float f32x16;
typedef __attribute__((ext_vector_type(2))) unsigned u32x2;
typedef __attribute__((ext_vector_type(4))) unsigned u32x4;
#define LDS_AS __attribute__((address_space(3)))
#define DEV __device__ __forceinline__

constexpr int SLOT = 40960, OFF_V = 12288, OFF_W = 24576, OFF_H = 122880;
constexpr int OFF_OA = 0, OFF_OB = 36864, OFF_WO = 73728, LDS_BYTES = 159744;
constexpr float SM_C = 0.07216878364870322f * 1.4426950408889634f;
constexpr float SM_THR = 8.0f / SM_C;

DEV void dma16(unsigned voff, const char* sbase, unsigned ldsdst) {
  asm volatile("s_mov_b32 m0, %2\n\ts_nop 0\n\tglobal_load_lds_dwordx4 %0, %1 nt" ::"v"(voff), "s"(sbase), "s"(ldsdst) : "memory");
}
template <int N> DEV void wait_vm() { asm volatile("s_waitcnt vmcnt(%0)" ::"n"(N) : "memory"); }
DEV void bar() { asm volatile("s_waitcnt lgkmcnt(0)\n\ts_barrier" ::: "memory"); }
DEV u32x4 lds_r128(unsigned a) { return *(LDS_AS const u32x4*)a; }
DEV void lds_w128(unsigned a, u32x4 v) { *(LDS_AS u32x4*)a = v; }
DEV bf16x8 lds_tr2(unsigned a0, unsigned a1) {
  s16x4 lo = __builtin_amdgcn_ds_read_tr16_b64_v4i16((LDS_AS s16x4*)a0);
  s16x4 hi = __builtin_amdgcn_ds_read_tr16_b64_v4i16((LDS_AS s16x4*)a1);
  s16x8 v = __builtin_shufflevector(lo, hi, 0, 1, 2, 3, 4, 5, 6, 7);
  return __builtin_bit_cast(bf16x8, v);
}
DEV unsigned pk2(float a, float b) {
  f32x2 f = {a, b};
  return __builtin_bit_cast(unsigned, __builtin_convertvector(f, bf16x2));
}
DEV u32x4 pk8(const f32x16& v, int o, float s) {
  u32x4 r = {pk2(v[o] * s, v[o + 1] * s), pk2(v[o + 2] * s, v[o + 3] * s), pk2(v[o + 4] * s, v[o + 5] * s), pk2(v[o + 6] * s, v[o + 7] * s)};
  return r;
}
DEV f32x16 mma32(bf16x8 a, bf16x8 b, f32x16 c) { return __builtin_amdgcn_mfma_f32_32x32x16_bf16(a, b, c, 0, 0, 0); }
DEV f32x4 mma16(bf16x8 a, bf16x8 b, f32x4 c) { return __builtin_amdgcn_mfma_f32_16x16x32_bf16(a, b, c, 0, 0, 0); }
DEV bf16x8 asbf(u32x4 v) { return __builtin_bit_cast(bf16x8, v); }
DEV float xhalf_max(float v) {
  u32x2 r = __builtin_amdgcn_permlane32_swap(__float_as_uint(v), __float_as_uint(v), false, false);
  return fmaxf(__uint_as_float(r.x), __uint_as_float(r.y));
}
DEV float xhalf_sum(float v) {
  u32x2 r = __builtin_amdgcn_permlane32_swap(__float_as_uint(v), __float_as_uint(v), false, false);
  return __uint_as_float(r.x) + __uint_as_float(r.y);
}
DEV unsigned next_slot(unsigned s) { return s == 2 * SLOT ? 0 : s + SLOT; }

template <bool ATT>
DEV void tail_oproj(unsigned L, int w, int l, const char* wo_h0, int rot, char* part, const u32x4* obf) {
  unsigned voff[5];
#pragma unroll
  for (int p = 0; p < 5; ++p) { int n = 64 * p + l; voff[p] = (n / 10) * 1280 + (n % 10) * 16 + w * 160; }
  const unsigned wbuf = L + OFF_WO + w * 5120;
  const int g = l >> 4, q = (l >> 2) & 3, pp = l & 3;
  const unsigned aaddr = wbuf + (16 * (g >> 1) + 4 * (g & 1) + q) * 160 + pp * 8;
  f32x4 acc[5][6];
#pragma unroll
  for (int j = 0; j < 5; ++j)
#pragma unroll
    for (int m = 0; m < 6; ++m) acc[j][m] = f32x4{0.f, 0.f, 0.f, 0.f};
  int ks = rot;
  {
    const char* src = wo_h0 + (2 * 192 + ks * 32) * 1280;
#pragma unroll
    for (int p = 0; p < 5; ++p) dma16(voff[p], src, wbuf + p * 1024);
  }
#pragma unroll 1
  for (int hi = 0; hi < 4; ++hi) {
    const unsigned obase = L + (hi == 1 ? OFF_OA : hi == 2 ? OFF_OB : OFF_H) + l * 16;
    if (hi == 1 || hi == 3) bar();
    if (ATT && hi == 1) {
      const unsigned ow = L + OFF_H + w * 12288 + ((l >> 4) & 1) * 1024 + (l >> 5) * 256 + (l & 15) * 16;
#pragma unroll
      for (int f = 0; f < 12; ++f) lds_w128(ow + (f >> 1) * 2048 + (f & 1) * 512, obf[f]);
    }
#pragma unroll 1
    for (int i = 0; i < 6; ++i) {
      bf16x8 bfr[6], afr[5];
#pragma unroll
      for (int m = 0; m < 6; ++m) bfr[m] = asbf(lds_r128(obase + ks * 2048 + (m >> 1) * 12288 + (m & 1) * 1024));
      wait_vm<0>();
#pragma unroll
      for (int j = 0; j < 5; ++j) afr[j] = lds_tr2(aaddr + j * 32, aaddr + j * 32 + 8 * 160);
      asm volatile("s_waitcnt lgkmcnt(0)" ::: "memory");
      ks = ks == 5 ? 0 : ks + 1;
      const int hn = i < 5 ? hi : hi + 1;
      if (hn < 4) {
        const int hh = hn == 0 ? 2 : hn == 3 ? 3 : hn - 1;
        const char* src = wo_h0 + (hh * 192 + ks * 32) * 1280;
#pragma unroll
        for (int p = 0; p < 5; ++p) dma16(voff[p], src, wbuf + p * 1024);
      }
#pragma unroll
      for (int j = 0; j < 5; ++j)
#pragma unroll
        for (int m = 0; m < 6; ++m) acc[j][m] = mma16(afr[j], bfr[m], acc[j][m]);
    }
  }
  char* dst = part + w * 15360 + l * 16;
#pragma unroll
  for (int j = 0; j < 5; ++j)
#pragma unroll
    for (int mp = 0; mp < 3; ++mp) {
      const f32x4 a = acc[j][2 * mp], b = acc[j][2 * mp + 1];
      u32x4 v = {pk2(a[0], a[1]), pk2(a[2], a[3]), pk2(b[0], b[1]), pk2(b[2], b[3])};
      __builtin_nontemporal_store(v, (u32x4*)(dst + (j * 3 + mp) * 1024));
    }
}

struct Att {
  f32x16 o[6];
  u32x4 q[12], p[2];
  float m, mc, lsum;
  unsigned ka[4], va[2];
};
DEV f32x16 att_qk(const Att& a, unsigned slot) {
  f32x16 s = {0.f, 0.f, 0.f, 0.f, 0.f, 0.f, 0.f, 0.f, 0.f, 0.f, 0.f, 0.f, 0.f, 0.f, 0.f, 0.f};
#pragma unroll
  for (int k = 0; k < 12; ++k) s = mma32(asbf(lds_r128(a.ka[k & 3] + slot + (k >> 2) * 128)), asbf(a.q[k]), s);
  return s;
}
template <int B0, int B1> DEV void att_pv(Att& a, unsigned slot) {
#pragma unroll
  for (int b = B0; b < B1; ++b)
#pragma unroll
    for (int s = 0; s < 2; ++s) {
      const unsigned ad = a.va[b & 1] + slot + (b >> 1) * 128 + s * 16 * 384;
      a.o[b] = mma32(lds_tr2(ad, ad + 8 * 384), asbf(a.p[s]), a.o[b]);
    }
}
DEV float max16(const f32x16& s) {
  float m0 = fmaxf(fmaxf(s[0], s[1]), s[2]), m1 = fmaxf(fmaxf(s[3], s[4]), s[5]);
  m0 = fmaxf(fmaxf(m0, s[6]), s[7]); m1 = fmaxf(fmaxf(m1, s[8]), s[9]);
  m0 = fmaxf(fmaxf(m0, s[10]), s[11]); m1 = fmaxf(fmaxf(m1, s[12]), s[13]);
  m0 = fmaxf(fmaxf(m0, s[14]), s[15]);
  return fmaxf(m0, m1);
}
DEV void att_exp(Att& a, f32x16 s, u32x4& p0, u32x4& p1) {
  float t0 = a.lsum, t1 = 0.f;
#pragma unroll
  for (int i = 0; i < 16; i += 2) {
    s[i] = __builtin_amdgcn_exp2f(__builtin_fmaf(s[i], SM_C, -a.mc));
    s[i + 1] = __builtin_amdgcn_exp2f(__builtin_fmaf(s[i + 1], SM_C, -a.mc));
    t0 += s[i]; t1 += s[i + 1];
  }
  a.lsum = t0 + t1;
  p0 = u32x4{pk2(s[0], s[1]), pk2(s[2], s[3]), pk2(s[4], s[5]), pk2(s[6], s[7])};
  p1 = u32x4{pk2(s[8], s[9]), pk2(s[10], s[11]), pk2(s[12], s[13]), pk2(s[14], s[15])};
}

struct Args { const char *x[2], *k[2], *v[2], *wq[2], *wo[2]; char* part; };

__global__ __launch_bounds__(512, 2) void fused_attn_kernel(Args args) {
  __shared__ __attribute__((aligned(1024))) char lds[LDS_BYTES];
  const unsigned L = (unsigned)(size_t)(LDS_AS char*)lds;
  const int w = __builtin_amdgcn_readfirstlane(threadIdx.x >> 6), l = threadIdx.x & 63;
  const int bid = blockIdx.x, xcd = bid & 7, ii = bid >> 3;
  const int br = xcd >> 2, b = ii & 15, hg = (xcd & 3) * 2 + (ii >> 4), h0 = hg * 4;
  const char* wo_h0 = (br ? args.wo[1] : args.wo[0]) + (size_t)h0 * 192 * 1280;
  char* part = args.part + (size_t)((br * 16 + b) * 8 + hg) * 122880;
  const int rot = b % 6;

  if (w < 3) {
    Att a;
    const int r = l & 31, h = l >> 5, g = l >> 4, q = (l >> 2) & 3, pp = l & 3;
#pragma unroll
    for (int s = 0; s < 4; ++s) a.ka[s] = L + r * 384 + (((2 * s + h) ^ ((r >> 1) & 7)) << 4);
#pragma unroll
    for (int e = 0; e < 2; ++e) a.va[e] = L + OFF_V + (4 * h + q) * 384 + ((e ^ (q >> 1)) << 6) + (g & 1) * 32 + pp * 8;
    const unsigned qaddr = L + OFF_H + w * 12288 + l * 16;
    const unsigned ow = L + OFF_H + w * 12288 + (g & 1) * 1024 + h * 256 + (l & 15) * 16;
#pragma unroll 1
    for (int t = 0; t < 16; ++t) bar();
#pragma unroll
    for (int f = 0; f < 12; ++f) a.q[f] = lds_r128(qaddr + f * 1024);
    unsigned slot = 0;
#pragma unroll 1
    for (int rd = 0; rd < 4; ++rd) {
      {
        bar();
        f32x16 s = att_qk(a, slot);
        if (rd > 0) {
          att_pv<0, 6>(a, slot);
          const float inv = 1.0f / xhalf_sum(a.lsum);
#pragma unroll
          for (int f = 0; f < 12; ++f) lds_w128(ow + (f >> 1) * 2048 + (f & 1) * 512, pk8(a.o[f >> 1], (f & 1) * 8, inv));
        }
#pragma unroll
        for (int bb = 0; bb < 6; ++bb)
#pragma unroll
          for (int i = 0; i < 16; ++i) a.o[bb][i] = 0.f;
        a.m = xhalf_max(max16(s));
        a.mc = a.m * SM_C;
        a.lsum = 0.f;
        att_exp(a, s, a.p[0], a.p[1]);
        slot = next_slot(slot);
      }
#pragma unroll 1
      for (int u = 1; u < 16; ++u) {
        bar();
        f32x16 s = att_qk(a, slot);
        att_pv<0, 2>(a, slot);
        const float mx = max16(s);
        const bool resc = __builtin_amdgcn_ballot_w64(mx > a.m + SM_THR) != 0;
        float alpha = 1.f;
        if (resc) {
          const float mn = fmaxf(a.m, xhalf_max(mx));
          alpha = __builtin_amdgcn_exp2f((a.m - mn) * SM_C);
          a.m = mn; a.mc = mn * SM_C; a.lsum *= alpha;
        }
        u32x4 p0, p1;
        att_exp(a, s, p0, p1);
        att_pv<2, 6>(a, slot);
        a.p[0] = p0; a.p[1] = p1;
        if (resc) {
#pragma unroll
          for (int bb = 0; bb < 6; ++bb) a.o[bb] *= alpha;
        }
        slot = next_slot(slot);
      }
      if (rd < 3) {
#pragma unroll
        for (int f = 0; f < 12; ++f) a.q[f] = lds_r128(qaddr + f * 1024);
      }
    }
    bar();
    att_pv<0, 6>(a, slot);
    const float inv = 1.0f / xhalf_sum(a.lsum);
    u32x4 obf[12];
#pragma unroll
    for (int f = 0; f < 12; ++f) obf[f] = pk8(a.o[f >> 1], (f & 1) * 8, inv);
    bar();
    tail_oproj<true>(L, w, l, wo_h0, rot, part, obf);
  } else if (w == 3 || w == 7) {
    const int isv = w >> 2;
    const char* kv = (isv ? (br ? args.v[1] : args.v[0]) : (br ? args.k[1] : args.k[0])) + (size_t)(b * 32 + h0) * 512 * 384;
    const char* wq = (br ? args.wq[1] : args.wq[0]) + h0 * 384 + (size_t)isv * 64 * 12288;
    unsigned koff[6];
#pragma unroll
    for (int p = 0; p < 6; ++p) {
      const int n = 64 * p + l, row = n / 24, c = n % 24;
      koff[p] = row * 384 + ((c ^ (isv ? ((row >> 1) & 1) << 2 : (row >> 1) & 7)) << 4);
    }
    const unsigned woff = (l >> 3) * 12288 + (((l & 7) ^ (((l >> 4) & 1) << 2)) << 4);
    unsigned slot = 2 * SLOT;
    auto beat = [&](int t) {
      const int tk = t - isv, tq = t + 16;
      const bool dokv = tk >= 0 && tk < 64, dow = tq < 64 && (tq & 15) < 15;
      if (dow) {
        const int u = tq & 15, third = u / 5, kb = u - third * 5;
        const char* src = wq + (tq >> 4) * 384 + third * 128 + (size_t)kb * 128 * 12288;
        const unsigned dst = L + slot + OFF_W + isv * 8192;
#pragma unroll
        for (int p = 0; p < 8; ++p) dma16(woff, src + p * 8 * 12288, dst + p * 1024);
      }
      if (dokv) {
        const char* src = kv + tk * 12288;
        const unsigned dst = L + slot + isv * OFF_V;
#pragma unroll
        for (int p = 0; p < 12; ++p) dma16(koff[p % 6], src + (p / 6) * 6144, dst + p * 1024);
      }
      if (dow) { if (dokv) wait_vm<20>(); else wait_vm<8>(); }
      else { if (dokv) wait_vm<12>(); else wait_vm<0>(); }
      slot = next_slot(slot);
    };
    beat(-16);
    beat(-15);
    u32x4 pk0[18], pk1[18];
    const unsigned haddr = L + OFF_H + isv * 18432 + l * 16;
#pragma unroll 1
    for (int t = -16; t <= 17; ++t) { bar(); beat(t + 2); }
#pragma unroll
    for (int f = 0; f < 18; ++f) pk0[f] = lds_r128(haddr + f * 1024);
#pragma unroll 1
    for (int t = 18; t <= 33; ++t) { bar(); beat(t + 2); }
#pragma unroll
    for (int f = 0; f < 18; ++f) pk1[f] = lds_r128(haddr + f * 1024);
#pragma unroll 1
    for (int t = 34; t <= 64; ++t) { bar(); beat(t + 2); }
    bar();
#pragma unroll
    for (int f = 0; f < 18; ++f) {
      lds_w128(L + OFF_OA + isv * 18432 + l * 16 + f * 1024, pk0[f]);
      lds_w128(L + OFF_OB + isv * 18432 + l * 16 + f * 1024, pk1[f]);
    }
    tail_oproj<false>(L, w, l, wo_h0, rot, part, nullptr);
  } else {
    const int a = w - 4, h = l >> 5, g = l >> 4, q = (l >> 2) & 3, pp = l & 3;
    const char* xp = (br ? args.x[1] : args.x[0]) + (size_t)(b * 96 + a * 32 + (l & 31)) * 1280 + h * 16;
    u32x4 xf[40];
#pragma unroll
    for (int s = 0; s < 40; ++s) xf[s] = __builtin_nontemporal_load((const u32x4*)(xp + s * 32));
    unsigned wa[2];
    const int ps = ((pp & 1) << 1) | (pp >> 1);
#pragma unroll
    for (int e = 0; e < 2; ++e) wa[e] = L + OFF_W + (8 * h + q) * 128 + ((e ^ (q >> 1)) << 6) + (g & 1) * 32 + ps * 8;
    const unsigned qaddr = L + OFF_H + a * 12288 + l * 16;
    unsigned slot = 2 * SLOT;
#pragma unroll 1
    for (int rd = 0; rd < 4; ++rd) {
#pragma unroll 1
      for (int third = 0; third < 3; ++third) {
        f32x16 acc[2];
#pragma unroll
        for (int i = 0; i < 16; ++i) { acc[0][i] = 0.f; acc[1][i] = 0.f; }
#pragma unroll
        for (int kb = 0; kb < 5; ++kb) {
          bar();
#pragma unroll
          for (int s = 0; s < 8; ++s)
#pragma unroll
            for (int e = 0; e < 2; ++e) {
              const unsigned ad = wa[e] + slot + s * 2048;
              acc[e] = mma32(lds_tr2(ad, ad + 512), asbf(xf[kb * 8 + s]), acc[e]);
            }
          slot = next_slot(slot);
        }
#pragma unroll
        for (int f = 0; f < 4; ++f) lds_w128(qaddr + (third * 4 + f) * 1024, pk8(acc[f >> 1], (f & 1) * 8, 1.0f));
      }
      bar();
      slot = next_slot(slot);
    }
#pragma unroll 1
    for (int t = 48; t <= 65; ++t) bar();
    tail_oproj<false>(L, w, l, wo_h0, rot, part, nullptr);
  }
}

__global__ __launch_bounds__(256) void reduce_kernel(const char* __restrict__ part, char* __restrict__ out) {
  const int idx = blockIdx.x * 256 + threadIdx.x;
  const int l = idx & 63, pc = (idx >> 6) % 120, bb = (idx >> 6) / 120;
  const char* src = part + (size_t)bb * 8 * 122880 + pc * 1024 + l * 16;
  u32x4 v[8];
#pragma unroll
  for (int i = 0; i < 8; ++i) v[i] = __builtin_nontemporal_load((const u32x4*)(src + i * 122880));
  float s[8];
#pragma unroll
  for (int e = 0; e < 4; ++e) {
    s[2 * e] = 0.f; s[2 * e + 1] = 0.f;
#pragma unroll
    for (int i = 0; i < 8; ++i) { s[2 * e] += __uint_as_float(v[i][e] << 16); s[2 * e + 1] += __uint_as_float(v[i][e] & 0xffff0000u); }
  }
  const int w = pc / 15, j = (pc % 15) / 3, mp = pc % 3, br = bb >> 4, b = bb & 15;
  char* dst = out + (size_t)(b * 96 + mp * 32 + (l & 15)) * 2560 + (br * 640 + w * 80 + j * 16 + (l >> 4) * 4) * 2;
  *(u32x2*)dst = u32x2{pk2(s[0], s[1]), pk2(s[2], s[3])};
  *(u32x2*)(dst + 16 * 2560) = u32x2{pk2(s[4], s[5]), pk2(s[6], s[7])};
}

extern "C" void kernel_launch(void* const* d_in, const int* in_sizes, int n_in, void* d_out, int out_size, void* d_ws,
                              size_t ws_size, hipStream_t stream) {
  Args a;
  for (int i = 0; i < 2; ++i) {
    a.x[i] = (const char*)d_in[5 * i]; a.k[i] = (const char*)d_in[5 * i + 1]; a.v[i] = (const char*)d_in[5 * i + 2];
    a.wq[i] = (const char*)d_in[5 * i + 3]; a.wo[i] = (const char*)d_in[5 * i + 4];
  }
  a.part = (char*)d_ws;
  fused_attn_kernel<<<256, 512, 0, stream>>>(a);
  reduce_kernel<<<960, 256, 0, stream>>>((const char*)d_ws, (char*)d_out);
}
```

```cpp
#include <hip/hip_runtime.h>
#include <hip/hip_bf16.h>

typedef __attribute__((ext_vector_type(8))) __bf16 bf16x8;
typedef __attribute__((ext_vector_type(2))) __bf16 bf16x2;
typedef __attribute__((ext_vector_type(4))) short s16x4;
typedef __attribute__((ext_vector_type(8))) short s16x8;
typedef __attribute__((ext_vector_type(2))) float f32x2;
typedef __attribute__((ext_vector_type(4))) float f32x4;
typedef __attribute__((ext_vector_type(16))) float f32x16;
typedef __attribute__((ext_vector_type(2))) unsigned u32x2;
typedef __attribute__((ext_vector_type(4))) unsigned u32x4;
#define LDS_AS __attribute__((address_space(3)))
#define DEV __device__ __forceinline__

constexpr int SLOT = 40960, OFF_V = 12288, OFF_W = 24576, OFF_H = 122880;
constexpr int OFF_OA = 0, OFF_OB = 36864, OFF_WO = 73728, OFF_DUMMY = 159744, LDS_BYTES = 163840;
constexpr float SM_C = 0.07216878364870322f * 1.4426950408889634f;
constexpr float SM_THR = 8.0f;

#define WO_NT 0
#define X_NT 0
#define WQ0_NT 0
#define WQ_PF 1
#define WO_PF 0
template <bool NT = true> DEV void dma16(unsigned voff, const char* sbase, unsigned ldsdst) {
  if (NT) asm volatile("s_mov_b32 m0, %2\n\ts_nop 0\n\tglobal_load_lds_dwordx4 %0, %1 nt" ::"v"(voff), "s"(sbase), "s"(ldsdst) : "memory");
  else asm volatile("s_mov_b32 m0, %2\n\ts_nop 0\n\tglobal_load_lds_dwordx4 %0, %1" ::"v"(voff), "s"(sbase), "s"(ldsdst) : "memory");
}
template <int N> DEV void wait_vm() { asm volatile("s_waitcnt vmcnt(%0)" ::"n"(N) : "memory"); }
DEV void bar() { asm volatile("s_waitcnt lgkmcnt(0)\n\ts_barrier" ::: "memory"); }
DEV u32x4 lds_r128(unsigned a) { return *(LDS_AS const u32x4*)a; }
DEV void lds_w128(unsigned a, u32x4 v) { *(LDS_AS u32x4*)a = v; }
DEV bf16x8 lds_tr2(unsigned a0, unsigned a1) {
  s16x4 lo = __builtin_amdgcn_ds_read_tr16_b64_v4i16((LDS_AS s16x4*)a0);
  s16x4 hi = __builtin_amdgcn_ds_read_tr16_b64_v4i16((LDS_AS s16x4*)a1);
  s16x8 v = __builtin_shufflevector(lo, hi, 0, 1, 2, 3, 4, 5, 6, 7);
  return __builtin_bit_cast(bf16x8, v);
}
DEV unsigned pk2(float a, float b) {
  f32x2 f = {a, b};
  return __builtin_bit_cast(unsigned, __builtin_convertvector(f, bf16x2));
}
DEV u32x4 pk8(const f32x16& v, int o, float s) {
  u32x4 r = {pk2(v[o] * s, v[o + 1] * s), pk2(v[o + 2] * s, v[o + 3] * s), pk2(v[o + 4] * s, v[o + 5] * s), pk2(v[o + 6] * s, v[o + 7] * s)};
  return r;
}
DEV f32x16 mma32(bf16x8 a, bf16x8 b, f32x16 c) { return __builtin_amdgcn_mfma_f32_32x32x16_bf16(a, b, c, 0, 0, 0); }
DEV f32x4 mma16(bf16x8 a, bf16x8 b, f32x4 c) { return __builtin_amdgcn_mfma_f32_16x16x32_bf16(a, b, c, 0, 0, 0); }
DEV bf16x8 asbf(u32x4 v) { return __builtin_bit_cast(bf16x8, v); }
DEV float xhalf_max(float v) {
  u32x2 r = __builtin_amdgcn_permlane32_swap(__float_as_uint(v), __float_as_uint(v), false, false);
  return fmaxf(__uint_as_float(r.x), __uint_as_float(r.y));
}
DEV float xhalf_sum(float v) {
  u32x2 r = __builtin_amdgcn_permlane32_swap(__float_as_uint(v), __float_as_uint(v), false, false);
  return __uint_as_float(r.x) + __uint_as_float(r.y);
}
#define STAMP(i)
#define BAR() bar()
DEV unsigned next_slot(unsigned s) { return s == 2 * SLOT ? 0 : s + SLOT; }

#define FENCE() __builtin_amdgcn_sched_barrier(0)
#define PIN(x) asm volatile("" : "+v"(x))
template <int NB, bool ATT>
DEV void tail_oproj(unsigned L, int w, int l, const char* wo_h0, int rot, char* part, const u32x4* obf) {
  constexpr int RB = NB * 64;
  const int pr = w & 3, nb0 = 5 * pr + ((pr & 1) ? (NB == 2 ? 3 : 0) : (NB == 2 ? 0 : 2));
  const int h = l >> 5, g = l >> 4, q = (l >> 2) & 3, ps = ((l & 1) << 1) | ((l >> 1) & 1);
  unsigned voff[NB], aa[NB];
  const unsigned wbuf = L + OFF_WO + pr * 10240 + (NB == 2 ? 0 : 4096);
#pragma unroll
  for (int p = 0; p < NB; ++p) {
    const int n = 64 * p + l, row = n / (4 * NB), c = n % (4 * NB);
    voff[p] = row * 1280 + nb0 * 64 + ((NB == 2 ? c ^ (((row >> 1) & 1) << 2) : c) << 4);
    aa[p] = wbuf + (4 * h + q) * RB + ((NB == 2 ? p ^ (q >> 1) : p) << 6) + (g & 1) * 32 + ps * 8;
  }
  auto kstep = [&](int n, int& hs) __attribute__((always_inline)) { hs = n / 12; int f = n - hs * 12 + rot; return f >= 12 ? f - 12 : f; };
  auto dma = [&](int n, int buf, int p) __attribute__((always_inline)) {
    int hs; const int f = kstep(n < 47 ? n : 47, hs), hh = hs == 0 ? 2 : hs == 3 ? 3 : hs - 1;
    dma16<false>(voff[p], wo_h0 + (hh * 192 + f * 16) * 1280, wbuf + buf * (NB * 1024) + p * 1024);
  };
  auto oadr = [&](int n) __attribute__((always_inline)) {
    int hs; const int f = kstep(n < 47 ? n : 47, hs);
    unsigned a = L + (hs == 1 ? OFF_OA : hs == 2 ? OFF_OB : OFF_H) + f * 1024 + l * 16;
    PIN(a);
    return a;
  };
  f32x16 acc[NB][3];
#pragma unroll
  for (int j = 0; j < NB; ++j)
#pragma unroll
    for (int m = 0; m < 3; ++m)
#pragma unroll
      for (int i = 0; i < 16; ++i) acc[j][m][i] = 0.f;
  bf16x8 af[2][NB], bf[2][3];
#pragma unroll
  for (int p = 0; p < 2 * NB; ++p) dma(p / NB, p / NB, p % NB);
  if (NB == 3) __builtin_amdgcn_s_setprio(1);
  wait_vm<NB>();
  {
    const unsigned oa = oadr(0);
#pragma unroll
    for (int j = 0; j < NB; ++j) af[0][j] = lds_tr2(aa[j], aa[j] + 8 * RB);
#pragma unroll
    for (int m = 0; m < 3; ++m) bf[0][m] = asbf(lds_r128(oa + m * 12288));
    asm volatile("s_waitcnt lgkmcnt(0)" ::: "memory");
#pragma unroll
    for (int p = 0; p < NB; ++p) dma(2, 0, p);
  }
  auto step = [&](int n, const int c) __attribute__((always_inline)) {
    const unsigned oa = oadr(n + 1);
    wait_vm<NB>();
    FENCE();
#pragma unroll
    for (int k = 0; k < 3 * NB; ++k) {
      const int j = k / 3, m = k % 3;
      acc[j][m] = mma32(af[c][j], bf[c][m], acc[j][m]);
      if (k < NB) af[c ^ 1][k] = lds_tr2(aa[k] + (c ^ 1) * (NB * 1024), aa[k] + (c ^ 1) * (NB * 1024) + 8 * RB);
      if (k < 3) bf[c ^ 1][k] = asbf(lds_r128(oa + k * 12288));
      if (k == 2 * NB) asm volatile("s_waitcnt lgkmcnt(0)" ::: "memory");
      if (k >= 2 * NB) dma(n + 3, c ^ 1, k - 2 * NB);
      FENCE();
    }
  };
#pragma unroll 1
  for (int n = 0; n < 48; n += 2) {
    if (n % 12 == 0) STAMP(6 + n / 12);
    step(n, 0);
    if (n == 10 || n == 34) bar();
    if (ATT && n == 10) {
#pragma unroll
      for (int f = 0; f < 12; ++f) lds_w128(L + OFF_H + w * 12288 + l * 16 + f * 1024, obf[f]);
    }
    step(n + 1, 1);
  }
  wait_vm<0>();
  STAMP(10);
  char* dst = part + nb0 * 6144 + l * 16;
#pragma unroll
  for (int j = 0; j < NB; ++j)
#pragma unroll
    for (int m = 0; m < 3; ++m)
#pragma unroll
      for (int s = 0; s < 2; ++s) __builtin_nontemporal_store(pk8(acc[j][m], s * 8, 1.0f), (u32x4*)(dst + ((j * 3 + m) * 2 + s) * 1024));
}

struct Att {
  f32x16 o[6];
  u32x4 q[12];
  float z[16], mc, lsum;
  unsigned ka[4], va[2];
};
struct Adr { unsigned k[4], v[2]; };
DEV Adr att_adr(const Att& a, unsigned slot) {
  Adr d;
#pragma unroll
  for (int i = 0; i < 4; ++i) { d.k[i] = a.ka[i] + slot; PIN(d.k[i]); }
#pragma unroll
  for (int i = 0; i < 2; ++i) { d.v[i] = a.va[i] + slot; PIN(d.v[i]); }
  return d;
}
DEV bf16x8 rd_k(const Adr& d, int k) { return asbf(lds_r128(d.k[k & 3] + (k >> 2) * 128)); }
DEV bf16x8 rd_v(const Adr& d, int i) {
  const unsigned ad = d.v[i & 1] + ((i % 6) >> 1) * 128 + (i / 6) * 16 * 384;
  return lds_tr2(ad, ad + 8 * 384);
}
DEV float max3(float a, float b, float c) { return fmaxf(fmaxf(a, b), c); }
template <bool QK, bool PV> DEV float att_beat(Att& a, unsigned slot) {
  const Adr d = att_adr(a, slot);
  f32x16 s = {0.f, 0.f, 0.f, 0.f, 0.f, 0.f, 0.f, 0.f, 0.f, 0.f, 0.f, 0.f, 0.f, 0.f, 0.f, 0.f};
  bf16x8 kf[12], vf[12];
  unsigned pn[8];
  float t0 = a.lsum, t1 = 0.f;
  auto ex = [&](int i) __attribute__((always_inline)) {
    const float e0 = __builtin_amdgcn_exp2f(a.z[2 * i]), e1 = __builtin_amdgcn_exp2f(a.z[2 * i + 1]);
    t0 += e0; PIN(t0); t1 += e1; PIN(t1);
    pn[i] = pk2(e0, e1); PIN(pn[i]);
  };
  if (QK) {
#pragma unroll
    for (int k = 0; k < 4; ++k) kf[k] = rd_k(d, k);
  }
  FENCE();
  if (PV) { ex(0); ex(1); }
  FENCE();
#pragma unroll
  for (int k = 0; k < 12; ++k) {
    if (QK) s = mma32(kf[k], asbf(a.q[k]), s);
    if (k < 8) { if (QK) kf[k + 4] = rd_k(d, k + 4); }
    else if (PV) vf[k - 8] = rd_v(d, k - 8);
    if (PV && k < 6) ex(k + 2);
    FENCE();
  }
  if (PV) a.lsum = t0 + t1;
  const bf16x8 p0 = asbf(u32x4{pn[0], pn[1], pn[2], pn[3]}), p1 = asbf(u32x4{pn[4], pn[5], pn[6], pn[7]});
  float mx = -3.0e38f;
#pragma unroll
  for (int i = 0; i < 12; ++i) {
    if (PV) {
      a.o[i % 6] = mma32(vf[i], i < 6 ? p0 : p1, a.o[i % 6]);
      if (i < 8) vf[i + 4] = rd_v(d, i + 4);
    }
    if (QK && i >= 3 && i < 11) {
      const int j = 2 * (i - 3);
      float z0 = __builtin_fmaf(s[j], SM_C, -a.mc), z1 = __builtin_fmaf(s[j + 1], SM_C, -a.mc);
      mx = max3(mx, z0, z1);
      PIN(z0); PIN(z1); PIN(mx);
      a.z[j] = z0; a.z[j + 1] = z1;
    }
    FENCE();
  }
  return mx;
}
DEV void att_shift(Att& a, float dl) {
  a.mc += dl;
#pragma unroll
  for (int i = 0; i < 16; ++i) a.z[i] -= dl;
}

struct Args { const char *x[2], *k[2], *v[2], *wq[2], *wo[2]; char* part; };

__global__ __launch_bounds__(512, 2) void fused_attn_kernel(Args args) {
  __shared__ __attribute__((aligned(1024))) char lds[LDS_BYTES];
  const unsigned L = (unsigned)(size_t)(LDS_AS char*)lds;
  const int w = __builtin_amdgcn_readfirstlane(threadIdx.x >> 6), l = threadIdx.x & 63;
  const int bid = blockIdx.x, xcd = bid & 7, ii = bid >> 3;
  const int br = xcd >> 2, b = ii & 15, hg = (xcd & 3) * 2 + (ii >> 4), h0 = hg * 4;
  const char* wo_h0 = (br ? args.wo[1] : args.wo[0]) + (size_t)h0 * 192 * 1280;
  char* part = args.part + (size_t)((br * 16 + b) * 8 + hg) * 122880;
  const int rot = b % 12;

  if (w < 3) {
    Att a;
    __builtin_amdgcn_s_setprio(1);
    STAMP(0);
    const int r = l & 31, h = l >> 5, g = l >> 4, q = (l >> 2) & 3, pp = l & 3;
#pragma unroll
    for (int s = 0; s < 4; ++s) a.ka[s] = L + r * 384 + (((2 * s + h) ^ ((r >> 1) & 7)) << 4);
#pragma unroll
    for (int e = 0; e < 2; ++e) a.va[e] = L + OFF_V + (4 * h + q) * 384 + ((e ^ (q >> 1)) << 6) + (g & 1) * 32 + pp * 8;
    const unsigned qaddr = L + OFF_H + w * 12288 + l * 16;
    {
      const char* src = (br ? args.wq[1] : args.wq[0]) + h0 * 384 + w * 128 + (size_t)b * 8 * 12288;
      const unsigned off = (l >> 3) * 12288 + (l & 7) * 16;
#pragma unroll
      for (int kb = 0; kb < 5; ++kb) dma16<WQ0_NT>(off, src + (size_t)kb * 128 * 12288, L + OFF_DUMMY + w * 1024);
    }
#pragma unroll 1
    for (int t = 0; t < 16; ++t) {
      BAR();
      if (t % 5 == 0) STAMP(12 + t / 5);
      if (WQ_PF && t >= 1) {
        const int hh = (t - 1) / 5, kb = (t - 1) % 5;
        const char* src = (br ? args.wq[1] : args.wq[0]) + (h0 + 1 + hh) * 384 + w * 128 + (size_t)(kb * 128 + b * 8) * 12288;
        dma16<false>((l >> 3) * 12288 + (l & 7) * 16, src, L + OFF_DUMMY + w * 1024);
      }
      if (WO_PF && t >= 5 && t < 15) {
        const char* src = wo_h0 + b * 61440 + w * 20480 + (t - 5) * 2048;
        dma16<false>(l * 16, src, L + OFF_DUMMY + w * 1024);
        dma16<false>(l * 16, src + 1024, L + OFF_DUMMY + w * 1024);
      }
    }
#pragma unroll
    for (int f = 0; f < 12; ++f) a.q[f] = lds_r128(qaddr + f * 1024);
    unsigned slot = 0;
    a.lsum = 0.f;
#pragma unroll 1
    for (int rd = 0; rd < 4; ++rd) {
      STAMP(1 + rd);
      {
        BAR();
        a.mc = 0.f;
        float mx;
        if (rd > 0) {
          mx = att_beat<true, true>(a, slot);
          const float inv = 1.0f / xhalf_sum(a.lsum);
#pragma unroll
          for (int f = 0; f < 12; ++f) lds_w128(qaddr + f * 1024, pk8(a.o[f >> 1], (f & 1) * 8, inv));
        } else {
          mx = att_beat<true, false>(a, slot);
        }
#pragma unroll
        for (int bb = 0; bb < 6; ++bb)
#pragma unroll
          for (int i = 0; i < 16; ++i) a.o[bb][i] = 0.f;
        a.lsum = 0.f;
        att_shift(a, xhalf_max(mx));
        slot = next_slot(slot);
      }
#pragma unroll 1
      for (int u = 1; u < 16; ++u) {
        BAR();
        const float mx = att_beat<true, true>(a, slot);
        if (__builtin_amdgcn_ballot_w64(mx > SM_THR) != 0) {
          const float dl = fmaxf(xhalf_max(mx), 0.f), alpha = __builtin_amdgcn_exp2f(-dl);
          att_shift(a, dl);
          a.lsum *= alpha;
#pragma unroll
          for (int bb = 0; bb < 6; ++bb) a.o[bb] *= alpha;
        }
        slot = next_slot(slot);
      }
      if (rd < 3) {
#pragma unroll
        for (int f = 0; f < 12; ++f) a.q[f] = lds_r128(qaddr + f * 1024);
      }
    }
    STAMP(5);
    BAR();
    att_beat<false, true>(a, slot);
    __builtin_amdgcn_s_setprio(0);
    const float inv = 1.0f / xhalf_sum(a.lsum);
    u32x4 obf[12];
#pragma unroll
    for (int f = 0; f < 12; ++f) obf[f] = pk8(a.o[f >> 1], (f & 1) * 8, inv);
    BAR();
    tail_oproj<2, true>(L, w, l, wo_h0, rot, part, obf);
    STAMP(11);
  } else if (w == 3 || w == 7) {
    const int isv = w >> 2;
    const char* kv = (isv ? (br ? args.v[1] : args.v[0]) : (br ? args.k[1] : args.k[0])) + (size_t)(b * 32 + h0) * 512 * 384;
    const char* wq = (br ? args.wq[1] : args.wq[0]) + h0 * 384 + (size_t)isv * 64 * 12288;
    unsigned koff[6];
#pragma unroll
    for (int p = 0; p < 6; ++p) {
      const int n = 64 * p + l, row = n / 24, c = n % 24;
      koff[p] = row * 384 + ((c ^ (isv ? ((row >> 1) & 1) << 2 : (row >> 1) & 7)) << 4);
    }
    const unsigned woff = (l >> 3) * 12288 + (((l & 7) ^ (((l >> 4) & 1) << 2)) << 4);
    const char* xs = (br ? args.x[1] : args.x[0]) + (size_t)(b * 96 + isv * 48) * 1280;
    unsigned xoff[4];
#pragma unroll
    for (int p = 0; p < 4; ++p) { const int row = 4 * p + (l >> 4); xoff[p] = row * 1280 + (((l & 15) ^ row) << 4); }
    unsigned slot = 2 * SLOT;
    auto beat = [&](int t) {
      const int tk = (t < 0 ? t + 9 : t) - isv, tq = t + 16;
      const bool dox = t < -11;
      const bool dokv = tk >= 0 && tk < 64 && (t < -6 || t > 2), dow = tq < 64 && (tq & 15) < 15;
      if (dox) {
        const char* src = xs + tq * 256;
        const unsigned dst = L + slot + isv * OFF_V;
#pragma unroll
        for (int p = 0; p < 12; ++p) dma16<X_NT>(xoff[p & 3], src + (p >> 2) * 16 * 1280, dst + p * 1024);
      }
      if (dow) {
        const int u = tq & 15, third = u / 5, kb = u - third * 5;
        const char* src = wq + (tq >> 4) * 384 + third * 128 + (size_t)kb * 128 * 12288;
        const unsigned dst = L + slot + OFF_W + isv * 8192;
        if (WQ_PF || (tq < 16 && !WQ0_NT)) {
#pragma unroll
          for (int p = 0; p < 8; ++p) dma16<false>(woff, src + p * 8 * 12288, dst + p * 1024);
        } else {
#pragma unroll
          for (int p = 0; p < 8; ++p) dma16(woff, src + p * 8 * 12288, dst + p * 1024);
        }
      }
      if (dokv) {
        const char* src = kv + tk * 12288;
        const unsigned dst = L + slot + isv * OFF_V;
#pragma unroll
        for (int p = 0; p < 12; ++p) dma16(koff[p % 6], src + (p / 6) * 6144, dst + p * 1024);
      }
      if (dow) { if (dokv || dox) wait_vm<20>(); else wait_vm<8>(); }
      else { if (dokv) wait_vm<12>(); else wait_vm<0>(); }
      slot = next_slot(slot);
    };
    beat(-16);
    beat(-15);
    u32x4 pk0[18], pk1[18];
    const unsigned haddr = L + OFF_H + isv * 18432 + l * 16;
#pragma unroll 1
    for (int t = -16; t <= 17; ++t) { BAR(); beat(t + 2); }
#pragma unroll
    for (int f = 0; f < 18; ++f) pk0[f] = lds_r128(haddr + f * 1024);
#pragma unroll 1
    for (int t = 18; t <= 33; ++t) { BAR(); beat(t + 2); }
#pragma unroll
    for (int f = 0; f < 18; ++f) pk1[f] = lds_r128(haddr + f * 1024);
#pragma unroll 1
    for (int t = 34; t <= 64; ++t) { BAR(); beat(t + 2); }
    BAR();
#pragma unroll
    for (int f = 0; f < 18; ++f) {
      lds_w128(L + OFF_OA + isv * 18432 + l * 16 + f * 1024, pk0[f]);
      lds_w128(L + OFF_OB + isv * 18432 + l * 16 + f * 1024, pk1[f]);
    }
    if (isv) tail_oproj<3, false>(L, w, l, wo_h0, rot, part, nullptr);
    else tail_oproj<2, false>(L, w, l, wo_h0, rot, part, nullptr);
  } else {
    const int a = w - 4, h = l >> 5, g = l >> 4, q = (l >> 2) & 3, pp = l & 3;
    u32x4 xf[40];
    const unsigned xa = L + (a * 32 + (l & 31)) * 256, xsw = l & 15;
    unsigned wa[2];
    const int ps = ((pp & 1) << 1) | (pp >> 1);
#pragma unroll
    for (int e = 0; e < 2; ++e) wa[e] = L + OFF_W + (8 * h + q) * 128 + ((e ^ (q >> 1)) << 6) + (g & 1) * 32 + ps * 8;
    const unsigned qaddr = L + OFF_H + a * 12288 + l * 16;
    unsigned slot = 2 * SLOT;
    auto qthird = [&](int third, const bool first) __attribute__((always_inline)) {
        f32x16 acc[2];
#pragma unroll
        for (int i = 0; i < 16; ++i) { acc[0][i] = 0.f; acc[1][i] = 0.f; }
        bf16x8 wf[84];
#pragma unroll
        for (int kb = 0; kb < 5; ++kb) {
          BAR();
          unsigned w0 = wa[0] + slot, w1 = wa[1] + slot;
          PIN(w0); PIN(w1);
          if (first) {
#pragma unroll
            for (int s = 0; s < 8; ++s) xf[kb * 8 + s] = lds_r128(xa + slot + (((2 * s + h) ^ xsw) << 4));
          }
#pragma unroll
          for (int i = 0; i < 16; ++i) {
            const int n = kb * 16 + i, c = n - 4;
            const unsigned ad = ((i & 1) ? w1 : w0) + (i >> 1) * 2048;
            if (i >= 4) acc[c & 1] = mma32(wf[c], asbf(xf[c >> 1]), acc[c & 1]);
            wf[n] = lds_tr2(ad, ad + 512);
            if (i < 4 && c >= 0) acc[c & 1] = mma32(wf[c], asbf(xf[c >> 1]), acc[c & 1]);
            FENCE();
          }
          slot = next_slot(slot);
        }
#pragma unroll
        for (int c = 76; c < 80; ++c) acc[c & 1] = mma32(wf[c], asbf(xf[c >> 1]), acc[c & 1]);
#pragma unroll
        for (int f = 0; f < 4; ++f) lds_w128(qaddr + (third * 4 + f) * 1024, pk8(acc[f >> 1], (f & 1) * 8, 1.0f));
    };
    qthird(0, true);
#pragma unroll 1
    for (int i = 1; i < 12; ++i) {
      const int third = i % 3;
      qthird(third, false);
      if (third == 2) { BAR(); slot = next_slot(slot); }
    }
#pragma unroll 1
    for (int t = 48; t <= 65; ++t) BAR();
    tail_oproj<3, false>(L, w, l, wo_h0, rot, part, nullptr);
  }
}

__global__ __launch_bounds__(256) void reduce_kernel(const char* __restrict__ part, char* __restrict__ out) {
  const int idx = blockIdx.x * 256 + threadIdx.x;
  const int l = idx & 63, pc = (idx >> 6) % 120, bb = (idx >> 6) / 120;
  const char* src = part + (size_t)bb * 8 * 122880 + pc * 1024 + l * 16;
  u32x4 v[8];
#pragma unroll
  for (int i = 0; i < 8; ++i) v[i] = __builtin_nontemporal_load((const u32x4*)(src + i * 122880));
  float s[8];
#pragma unroll
  for (int e = 0; e < 4; ++e) {
    s[2 * e] = 0.f; s[2 * e + 1] = 0.f;
#pragma unroll
    for (int i = 0; i < 8; ++i) { s[2 * e] += __uint_as_float(v[i][e] << 16); s[2 * e + 1] += __uint_as_float(v[i][e] & 0xffff0000u); }
  }
  const int nb = pc / 6, m = (pc % 6) >> 1, sh = pc & 1, br = bb >> 4, b = bb & 15;
  char* dst = out + (size_t)(b * 96 + m * 32 + (l & 31)) * 2560 + (br * 640 + nb * 32 + sh * 16 + (l >> 5) * 8) * 2;
  *(u32x4*)dst = u32x4{pk2(s[0], s[1]), pk2(s[2], s[3]), pk2(s[4], s[5]), pk2(s[6], s[7])};
}

extern "C" void kernel_launch(void* const* d_in, const int* in_sizes, int n_in, void* d_out, int out_size, void* d_ws,
                              size_t ws_size, hipStream_t stream) {
  Args a;
  for (int i = 0; i < 2; ++i) {
    a.x[i] = (const char*)d_in[5 * i]; a.k[i] = (const char*)d_in[5 * i + 1]; a.v[i] = (const char*)d_in[5 * i + 2];
    a.wq[i] = (const char*)d_in[5 * i + 3]; a.wo[i] = (const char*)d_in[5 * i + 4];
  }
  a.part = (char*)d_ws;
  fused_attn_kernel<<<256, 512, 0, stream>>>(a);
  reduce_kernel<<<960, 256, 0, stream>>>((const char*)d_ws, (char*)d_out);
}
```

```cpp
#include <hip/hip_runtime.h>
#include <hip/hip_bf16.h>

typedef __attribute__((ext_vector_type(8))) __bf16 bf16x8;
typedef __attribute__((ext_vector_type(2))) __bf16 bf16x2;
typedef __attribute__((ext_vector_type(4))) short s16x4;
typedef __attribute__((ext_vector_type(8))) short s16x8;
typedef __attribute__((ext_vector_type(2))) float f32x2;
typedef __attribute__((ext_vector_type(16))) float f32x16;
typedef __attribute__((ext_vector_type(2))) unsigned u32x2;
typedef __attribute__((ext_vector_type(4))) unsigned u32x4;
#define LDS_AS __attribute__((address_space(3)))
#define DEV __device__ __forceinline__

constexpr int SLOT = 40960, OFF_V = 12288, OFF_W = 24576, OFF_H = 122880;
constexpr int OFF_DUMMY = 159744, LDS_BYTES = 163840;
constexpr float SM_C = 0.07216878364870322f * 1.4426950408889634f;
constexpr float SM_THR = 8.0f;

DEV void dma16(unsigned voff, const char* sbase, unsigned ldsdst) {
  asm volatile("s_mov_b32 m0, %2\n\ts_nop 0\n\tglobal_load_lds_dwordx4 %0, %1" ::"v"(voff), "s"(sbase), "s"(ldsdst) : "memory");
}
template <int D> DEV void dma4(const unsigned* v, const char* sbase, unsigned ldsdst) {
  asm volatile("s_add_u32 m0, %5, %6\n\ts_nop 0\n\tglobal_load_lds_dwordx4 %0, %4\n\tglobal_load_lds_dwordx4 %1, %4 offset:1024\n\t"
               "global_load_lds_dwordx4 %2, %4 offset:2048\n\tglobal_load_lds_dwordx4 %3, %4 offset:3072"
               ::"v"(v[0]), "v"(v[1]), "v"(v[2]), "v"(v[3]), "s"(sbase), "s"(ldsdst), "n"(D) : "memory", "scc");
}
template <int D> DEV void dma3nt(const unsigned* v, const char* sbase, unsigned ldsdst) {
  asm volatile("s_add_u32 m0, %4, %5\n\ts_nop 0\n\tglobal_load_lds_dwordx4 %0, %3 nt\n\tglobal_load_lds_dwordx4 %1, %3 offset:1024 nt\n\t"
               "global_load_lds_dwordx4 %2, %3 offset:2048 nt"
               ::"v"(v[0]), "v"(v[1]), "v"(v[2]), "s"(sbase), "s"(ldsdst), "n"(D) : "memory", "scc");
}
DEV void dma_kv(const unsigned* v, const char* src, unsigned dst) {
  dma3nt<0>(v, src, dst); dma3nt<3072>(v + 3, src, dst);
  dma3nt<6144>(v, src + 6144, dst); dma3nt<9216>(v + 3, src + 6144, dst);
}
template <int N> DEV void wait_vm() { asm volatile("s_waitcnt vmcnt(%0)" ::"n"(N) : "memory"); }
DEV void bar() { asm volatile("s_waitcnt lgkmcnt(0)\n\ts_barrier" ::: "memory"); }
DEV u32x4 lds_r128(unsigned a) { return *(LDS_AS const u32x4*)a; }
DEV void lds_w128(unsigned a, u32x4 v) { *(LDS_AS u32x4*)a = v; }
DEV bf16x8 lds_tr2(unsigned a0, unsigned a1) {
  s16x4 lo = __builtin_amdgcn_ds_read_tr16_b64_v4i16((LDS_AS s16x4*)a0);
  s16x4 hi = __builtin_amdgcn_ds_read_tr16_b64_v4i16((LDS_AS s16x4*)a1);
  s16x8 v = __builtin_shufflevector(lo, hi, 0, 1, 2, 3, 4, 5, 6, 7);
  return __builtin_bit_cast(bf16x8, v);
}
DEV unsigned pk2(float a, float b) {
  f32x2 f = {a, b};
  return __builtin_bit_cast(unsigned, __builtin_convertvector(f, bf16x2));
}
DEV u32x4 pk8(const f32x16& v, int o, float s) {
  u32x4 r = {pk2(v[o] * s, v[o + 1] * s), pk2(v[o + 2] * s, v[o + 3] * s), pk2(v[o + 4] * s, v[o + 5] * s), pk2(v[o + 6] * s, v[o + 7] * s)};
  return r;
}
DEV f32x16 mma32(bf16x8 a, bf16x8 b, f32x16 c) { return __builtin_amdgcn_mfma_f32_32x32x16_bf16(a, b, c, 0, 0, 0); }
DEV bf16x8 asbf(u32x4 v) { return __builtin_bit_cast(bf16x8, v); }
DEV float xhalf_max(float v) {
  u32x2 r = __builtin_amdgcn_permlane32_swap(__float_as_uint(v), __float_as_uint(v), false, false);
  return fmaxf(__uint_as_float(r.x), __uint_as_float(r.y));
}
DEV float xhalf_sum(float v) {
  u32x2 r = __builtin_amdgcn_permlane32_swap(__float_as_uint(v), __float_as_uint(v), false, false);
  return __uint_as_float(r.x) + __uint_as_float(r.y);
}
DEV unsigned next_slot(unsigned s) { return s == 2 * SLOT ? 0 : s + SLOT; }

#define FENCE() __builtin_amdgcn_sched_barrier(0)
#define PIN(x) asm volatile("" : "+v"(x))
DEV unsigned early_off(int e) { return e == 0 ? OFF_W + 12288 : e == 7 ? OFF_DUMMY : (e < 4 ? SLOT : 2 * SLOT - 9216) + OFF_W + 3072 + e * 3072; }
DEV void wo_warm(const char* wo_h0, int b, int i, int l, unsigned dst) {
  const int off = i < 10 ? (i < 5 ? 160 + i : 395 + i) * 1024 + b * 5120 : 2 * 245760 + b * 15360 + (i < 25 ? i - 10 : 0) * 1024;
  dma16(l * 16, wo_h0 + off, dst);
}
DEV unsigned own_off(unsigned k) { return (k < 9 ? SLOT : 2 * SLOT - 9216) + OFF_W + 6144 + k * 1024; }
template <int NB> struct Tail {
  unsigned voff[NB], aa[NB], wbuf, tabw, tabo;
  const char* wo;
  int nb0;
  DEV const char* wsrc(int n) const { return wo + (unsigned)__builtin_amdgcn_readlane(tabw, n); }
  DEV void dma(const char* src, int buf, int p) const { dma16(voff[p], src, wbuf + buf * (NB * 1024) + p * 1024); }
};
template <int NB> DEV Tail<NB> tail_init(unsigned L, int w, int l, const char* wo_h0, int b) {
  Tail<NB> t;
  constexpr int RB = NB * 64;
  const int pr = w & 3;
  t.nb0 = 5 * pr + ((pr & 1) ? (NB == 2 ? 3 : 0) : (NB == 2 ? 0 : 2));
  const int h = l >> 5, g = l >> 4, q = (l >> 2) & 3, ps = ((l & 1) << 1) | ((l >> 1) & 1);
  t.wbuf = L + (NB == 2 ? 2 * SLOT + pr * 4096 : OFF_W + (pr < 2 ? pr * 6144 : (pr - 1) * SLOT));
  t.wo = wo_h0;
#pragma unroll
  for (int p = 0; p < NB; ++p) {
    const int n = 64 * p + l, row = n / (4 * NB), c = n % (4 * NB);
    t.voff[p] = row * 1280 + t.nb0 * 64 + ((NB == 2 ? c ^ (((row >> 1) & 1) << 2) : c) << 4);
    t.aa[p] = t.wbuf + (4 * h + q) * RB + ((NB == 2 ? p ^ (q >> 1) : p) << 6) + (g & 1) * 32 + ps * 8;
  }
  const int n = l < 47 ? l : 47, e = (n + 4 + b) & 7, f12 = (n + b) % 12;
  int hd, f;
  if (n < 12 || n >= 36) { hd = n < 12 ? 2 : 3; f = f12; t.tabo = OFF_H + f * 3072; }
  else if (n < 20) { hd = e >> 2; f = 8 + (e & 3); t.tabo = early_off(e); }
  else { hd = n >= 28; f = e; t.tabo = hd * SLOT + f * 3072; }
  t.tabo += L;
  t.tabw = (hd * 192 + f * 16) * 1280;
#pragma unroll
  for (int p = 0; p < 2 * NB; ++p) t.dma(t.wsrc(p / NB), p / NB, p % NB);
  return t;
}
template <int NB, bool ATT>
DEV void tail_run(const Tail<NB>& t, unsigned L, int w, int l, char* part, int own0, const u32x4* obf) {
  constexpr int RB = NB * 64;
  auto oadr = [&](int n) __attribute__((always_inline)) {
    unsigned a = (unsigned)__builtin_amdgcn_readlane(t.tabo, n) + l * 16;
    PIN(a);
    return a;
  };
  f32x16 acc[NB][3];
  const f32x16 zero = {0.f, 0.f, 0.f, 0.f, 0.f, 0.f, 0.f, 0.f, 0.f, 0.f, 0.f, 0.f, 0.f, 0.f, 0.f, 0.f};
  bf16x8 af[2][NB], bf[2][3];
  if (NB == 3) { bar(); bar(); }
  wait_vm<NB>();
  {
    const unsigned oa = oadr(0);
#pragma unroll
    for (int j = 0; j < NB; ++j) af[0][j] = lds_tr2(t.aa[j], t.aa[j] + 8 * RB);
#pragma unroll
    for (int m = 0; m < 3; ++m) bf[0][m] = asbf(lds_r128(oa + m * 1024));
    asm volatile("s_waitcnt lgkmcnt(0)" ::: "memory");
#pragma unroll
    for (int p = 0; p < NB; ++p) t.dma(t.wsrc(2), 0, p);
  }
  auto step = [&](int n, const int c, const bool first) __attribute__((always_inline)) {
    unsigned oa;
    const char* src;
    wait_vm<NB>();
    FENCE();
#pragma unroll
    for (int k = 0; k < 3 * NB; ++k) {
      const int j = k / 3, m = k % 3;
      acc[j][m] = mma32(af[c][j], bf[c][m], first ? zero : acc[j][m]);
      auto rda = [&](int j2) __attribute__((always_inline)) {
        const unsigned ad = t.aa[j2] + (c ^ 1) * (NB * 1024);
        af[c ^ 1][j2] = lds_tr2(ad, ad + 8 * RB);
      };
      if (k == 0) { rda(0); rda(1); }
      if (k == 1 && NB == 3) rda(2);
      if (k == NB - 1) oa = oadr(n + 1);
      if (k == 2 * NB - 1) { src = t.wsrc(n + 3); asm volatile("" : "+s"(src)); }
      if (k >= NB - 1 && k < NB + 2) bf[c ^ 1][k - NB + 1] = asbf(lds_r128(oa + (k - NB + 1) * 1024));
      if (k == 2 * NB) asm volatile("s_waitcnt lgkmcnt(3)" ::: "memory");
      if (k >= 2 * NB) t.dma(src, c ^ 1, k - 2 * NB);
      FENCE();
    }
  };
  constexpr unsigned long long bm = NB == 2 ? (1ull << 10) | (1ull << 22) : 0x5f7dfull | (1ull << 26) | (1ull << 32);
  step(0, 0, true);
  if (ATT) __builtin_amdgcn_s_setprio(0);
  if (NB == 3) bar();
  step(1, 1, false);
  if (NB == 3) bar();
#pragma unroll 1
  for (int n = 2; n < 48; n += 2) {
    step(n, 0, false);
    if ((bm >> n) & 1) {
      bar();
      if (ATT && n == 10)
#pragma unroll
      for (int f = 0; f < 12; ++f) lds_w128(L + OFF_H + w * 1024 + l * 16 + f * 3072, obf[f]);
    }
    step(n + 1, 1, false);
    if (NB == 3 && ((bm >> n) & 2)) bar();
  }
  wait_vm<0>();
  char* dst = part + t.nb0 * 6144 + l * 16;
#pragma unroll
  for (int j = 0; j < NB; ++j)
#pragma unroll
    for (int m = 0; m < 3; ++m)
#pragma unroll
      for (int s = 0; s < 2; ++s) {
        const int i = (j * 3 + m) * 2 + s;
        const unsigned k = t.nb0 * 6 + i - own0;
        const u32x4 v = pk8(acc[j][m], s * 8, 1.0f);
        if (k < 15) lds_w128(L + own_off(k) + l * 16, v);
        else asm volatile("global_store_dwordx4 %0, %1, off sc0 sc1" ::"v"(dst + i * 1024), "v"(v) : "memory");
      }
}

struct Att {
  f32x16 o[6];
  u32x4 q[12];
  float z[16], mc, lsum;
  unsigned ka[4], va[2];
};
struct Adr { unsigned k[4], v[2]; };
DEV Adr att_adr(const Att& a, unsigned slot) {
  Adr d;
#pragma unroll
  for (int i = 0; i < 4; ++i) { d.k[i] = a.ka[i] + slot; PIN(d.k[i]); }
#pragma unroll
  for (int i = 0; i < 2; ++i) { d.v[i] = a.va[i] + slot; PIN(d.v[i]); }
  return d;
}
DEV bf16x8 rd_k(const Adr& d, int k) { return asbf(lds_r128(d.k[k & 3] + (k >> 2) * 128)); }
DEV bf16x8 rd_v(const Adr& d, int i) {
  const unsigned ad = d.v[i & 1] + ((i % 6) >> 1) * 128 + (i / 6) * 16 * 384;
  return lds_tr2(ad, ad + 8 * 384);
}
DEV float max3(float a, float b, float c) { return fmaxf(fmaxf(a, b), c); }
template <bool QK, bool PV> DEV float att_beat(Att& a, unsigned slot) {
  const Adr d = att_adr(a, slot);
  f32x16 s = {0.f, 0.f, 0.f, 0.f, 0.f, 0.f, 0.f, 0.f, 0.f, 0.f, 0.f, 0.f, 0.f, 0.f, 0.f, 0.f};
  bf16x8 kf[12], vf[12];
  unsigned pn[8];
  float t0 = a.lsum, t1 = 0.f;
  auto ex = [&](int i) __attribute__((always_inline)) {
    const float e0 = __builtin_amdgcn_exp2f(a.z[2 * i]), e1 = __builtin_amdgcn_exp2f(a.z[2 * i + 1]);
    t0 += e0; PIN(t0); t1 += e1; PIN(t1);
    pn[i] = pk2(e0, e1); PIN(pn[i]);
  };
  if (QK) {
#pragma unroll
    for (int k = 0; k < 4; ++k) kf[k] = rd_k(d, k);
  }
  FENCE();
  if (PV) { ex(0); ex(1); }
  FENCE();
#pragma unroll
  for (int k = 0; k < 12; ++k) {
    if (QK) s = mma32(kf[k], asbf(a.q[k]), s);
    if (k < 8) { if (QK) kf[k + 4] = rd_k(d, k + 4); }
    else if (PV) vf[k - 8] = rd_v(d, k - 8);
    if (PV && k < 6) ex(k + 2);
    FENCE();
  }
  if (PV) a.lsum = t0 + t1;
  const bf16x8 p0 = asbf(u32x4{pn[0], pn[1], pn[2], pn[3]}), p1 = asbf(u32x4{pn[4], pn[5], pn[6], pn[7]});
  float mx = -3.0e38f;
#pragma unroll
  for (int i = 0; i < 12; ++i) {
    if (PV) {
      a.o[i % 6] = mma32(vf[i], i < 6 ? p0 : p1, a.o[i % 6]);
      if (i < 8) vf[i + 4] = rd_v(d, i + 4);
    }
    if (QK && i >= 3 && i < 11) {
      const int j = 2 * (i - 3);
      float z0 = __builtin_fmaf(s[j], SM_C, -a.mc), z1 = __builtin_fmaf(s[j + 1], SM_C, -a.mc);
      mx = max3(mx, z0, z1);
      PIN(z0); PIN(z1); PIN(mx);
      a.z[j] = z0; a.z[j + 1] = z1;
    }
    FENCE();
  }
  return mx;
}
DEV void att_shift(Att& a, float dl) {
  a.mc += dl;
#pragma unroll
  for (int i = 0; i < 16; ++i) a.z[i] -= dl;
}

struct Args { const char *x[2], *k[2], *v[2], *wq[2], *wo[2]; char *part, *out; };
typedef __attribute__((address_space(1))) unsigned gu32;
#define RLX_AGENT __ATOMIC_RELAXED, __HIP_MEMORY_SCOPE_AGENT

__global__ __launch_bounds__(512, 2) void fused_attn_kernel(Args args) {
  __shared__ __attribute__((aligned(1024))) char lds[LDS_BYTES];
  const unsigned L = (unsigned)(size_t)(LDS_AS char*)lds;
  const int w = __builtin_amdgcn_readfirstlane(threadIdx.x >> 6), l = threadIdx.x & 63;
  const int bid = blockIdx.x, xcd = bid & 7, ii = bid >> 3;
  const int br = xcd >> 2, b = ii & 15, hg = (xcd & 3) * 2 + (ii >> 4), h0 = hg * 4;
  const char* wo_h0 = (br ? args.wo[1] : args.wo[0]) + (size_t)h0 * 192 * 1280;
  char* part = args.part + (size_t)((br * 16 + b) * 8 + hg) * 122880;

  gu32* mail = (gu32*)(args.part + 256 * 122880 + (br * 16 + b) * 2048);
  unsigned expect = 0, sent = 0;

  if (w < 3) {
    Att a;
    __builtin_amdgcn_s_setprio(1);
    const int r = l & 31, h = l >> 5, g = l >> 4, q = (l >> 2) & 3, pp = l & 3;
#pragma unroll
    for (int s = 0; s < 4; ++s) a.ka[s] = L + r * 384 + (((2 * s + h) ^ ((r >> 1) & 7)) << 4);
#pragma unroll
    for (int e = 0; e < 2; ++e) a.va[e] = L + OFF_V + (4 * h + q) * 384 + ((e ^ (q >> 1)) << 6) + (g & 1) * 32 + pp * 8;
    const unsigned qaddr = L + OFF_H + w * 1024 + l * 16;
    {
      const char* src = (br ? args.wq[1] : args.wq[0]) + h0 * 384 + w * 128 + (size_t)b * 8 * 12288;
      const unsigned off = (l >> 3) * 12288 + (l & 7) * 16;
#pragma unroll
      for (int kb = 0; kb < 5; ++kb) dma16(off, src + (size_t)kb * 128 * 12288, L + OFF_DUMMY + w * 1024);
    }
#pragma unroll 1
    for (int t = 0; t < 16; ++t) {
      bar();
      if (t >= 7) wo_warm(wo_h0, b, 3 * (t - 7) + w, l, L + OFF_DUMMY + w * 1024);
      if (t >= 1) {
        const int hh = (t - 1) / 5, kb = (t - 1) % 5;
        const char* src = (br ? args.wq[1] : args.wq[0]) + (h0 + 1 + hh) * 384 + w * 128 + (size_t)(kb * 128 + b * 8) * 12288;
        dma16((l >> 3) * 12288 + (l & 7) * 16, src, L + OFF_DUMMY + w * 1024);
      }
    }
#pragma unroll
    for (int f = 0; f < 12; ++f) a.q[f] = lds_r128(qaddr + f * 3072);
    unsigned slot = 0;
    a.lsum = 0.f;
#pragma unroll 1
    for (int rd = 0; rd < 4; ++rd) {
      {
        bar();
        a.mc = 0.f;
        float mx;
        if (rd > 0) {
          mx = att_beat<true, true>(a, slot);
          const float inv = 1.0f / xhalf_sum(a.lsum);
#pragma unroll
          for (int f = 0; f < 12; ++f) lds_w128(qaddr + f * 3072, pk8(a.o[f >> 1], (f & 1) * 8, inv));
        } else {
          mx = att_beat<true, false>(a, slot);
        }
#pragma unroll
        for (int bb = 0; bb < 6; ++bb)
#pragma unroll
          for (int i = 0; i < 16; ++i) a.o[bb][i] = 0.f;
        a.lsum = 0.f;
        att_shift(a, xhalf_max(mx));
        slot = next_slot(slot);
      }
#pragma unroll 1
      for (int u = 1; u < 16; ++u) {
        bar();
        const float mx = att_beat<true, true>(a, slot);
        if (__builtin_amdgcn_ballot_w64(mx > SM_THR) != 0) {
          const float dl = fmaxf(xhalf_max(mx), 0.f), alpha = __builtin_amdgcn_exp2f(-dl);
          att_shift(a, dl);
          a.lsum *= alpha;
#pragma unroll
          for (int bb = 0; bb < 6; ++bb) a.o[bb] *= alpha;
        }
        slot = next_slot(slot);
      }
      if (rd < 3) {
        wait_vm<0>();
#pragma unroll
        for (int f = 0; f < 12; ++f) a.q[f] = lds_r128(qaddr + f * 3072);
      }
    }
    const Tail<2> tl = tail_init<2>(L, w, l, wo_h0, b);
    bar();
    att_beat<false, true>(a, slot);
    bar();
    const float inv = 1.0f / xhalf_sum(a.lsum);
    u32x4 obf[12];
#pragma unroll
    for (int f = 0; f < 12; ++f) obf[f] = pk8(a.o[f >> 1], (f & 1) * 8, inv);
    tail_run<2, true>(tl, L, w, l, part, hg * 15, obf);
  } else if (w == 3 || w == 7) {
    const int isv = w >> 2;
    const char* kv = (isv ? (br ? args.v[1] : args.v[0]) : (br ? args.k[1] : args.k[0])) + (size_t)(b * 32 + h0) * 512 * 384;
    const char* wq = (br ? args.wq[1] : args.wq[0]) + h0 * 384 + (size_t)isv * 64 * 12288;
    unsigned koff[6];
#pragma unroll
    for (int p = 0; p < 6; ++p) {
      const int n = 64 * p + l, row = n / 24, c = n % 24;
      koff[p] = row * 384 + ((c ^ (isv ? ((row >> 1) & 1) << 2 : (row >> 1) & 7)) << 4) - (p % 3) * 1024;
    }
    unsigned woff[4], xoff[4];
#pragma unroll
    for (int p = 0; p < 4; ++p) {
      const int row = 4 * p + (l >> 4);
      xoff[p] = row * 1280 + (((l & 15) ^ row) << 4) - p * 1024;
      woff[p] = (8 * p + (l >> 3)) * 12288 + (((l & 7) ^ (((l >> 4) & 1) << 2)) << 4) - p * 1024;
    }
    const char* xs = (br ? args.x[1] : args.x[0]) + (size_t)(b * 96 + isv * 48) * 1280;
    unsigned slot = 2 * SLOT;
    int kb = 0;
    auto wtile = [&]() __attribute__((always_inline)) {
      const unsigned dst = L + slot + isv * 8192;
      dma4<OFF_W>(woff, wq, dst);
      dma4<OFF_W + 4096>(woff, wq + 32 * 12288, dst);
      wq += kb == 4 ? 128 - 4 * 128 * 12288 : 128 * 12288;
      kb = kb == 4 ? 0 : kb + 1;
    };
#pragma unroll 1
    for (int t = -16; t < -11; ++t) {
      if (t > -15) bar();
      const unsigned dst = L + slot + isv * OFF_V;
      dma4<0>(xoff, xs, dst); dma4<4096>(xoff, xs + 16 * 1280, dst); dma4<8192>(xoff, xs + 32 * 1280, dst);
      xs += 256;
      wtile();
      wait_vm<20>();
      slot = next_slot(slot);
    }
    unsigned long long mw = 0x03fffbfffbfffbffull, mk = ~0x3fffull | (isv ? 0x18 : 0x1c);
    auto beat = [&]() __attribute__((always_inline)) {
      bar();
      if (mw & 1) wtile();
      if (mk & 1) {
        dma_kv(koff, kv, L + slot + isv * OFF_V);
        kv += 12288;
        if (mw & 1) wait_vm<20>(); else wait_vm<12>();
      } else {
        if (mw & 1) wait_vm<8>(); else wait_vm<0>();
      }
      mw >>= 1; mk >>= 1;
      slot = next_slot(slot);
    };
#pragma unroll 1
    for (int t = -13; t <= 17; ++t) beat();
    if (isv) {
      u32x4 pk[24];
#pragma unroll
      for (int f = 0; f < 12; ++f) pk[f] = lds_r128(L + OFF_H + 24576 + l * 16 + f * 1024);
#pragma unroll 1
      for (int t = 18; t <= 33; ++t) beat();
#pragma unroll
      for (int f = 0; f < 12; ++f) pk[12 + f] = lds_r128(L + OFF_H + 24576 + l * 16 + f * 1024);
#pragma unroll 1
      for (int t = 34; t <= 46; ++t) beat();
      bar();
#pragma unroll
      for (int f = 0; f < 24; ++f) lds_w128(L + early_off(f / 3) + (f % 3) * 1024 + l * 16, pk[f]);
      const Tail<3> tl = tail_init<3>(L, w, l, wo_h0, b);
      wait_vm<6>();
      tail_run<3, false>(tl, L, w, l, part, hg * 15, nullptr);
    } else {
      u32x4 pk0[24], pk1[24];
#pragma unroll
      for (int f = 0; f < 24; ++f) pk0[f] = lds_r128(L + OFF_H + l * 16 + f * 1024);
#pragma unroll 1
      for (int t = 18; t <= 33; ++t) beat();
#pragma unroll
      for (int f = 0; f < 24; ++f) pk1[f] = lds_r128(L + OFF_H + l * 16 + f * 1024);
#pragma unroll 1
      for (int t = 34; t <= 46; ++t) beat();
      const char* vv = (br ? args.v[1] : args.v[0]) + (size_t)(b * 32 + h0) * 512 * 384 + 48 * 12288;
      unsigned voff[6];
#pragma unroll
      for (int p = 0; p < 6; ++p) {
        const int n = 64 * p + l, row = n / 24, c = n % 24;
        voff[p] = row * 384 + ((c ^ (((row >> 1) & 1) << 2)) << 4) - (p % 3) * 1024;
      }
#pragma unroll 1
      for (int t = 49; t <= 64; ++t) {
        bar();
        dma_kv(voff, vv, L + slot + OFF_V);
        vv += 12288;
        if (t < 64) {
          dma_kv(koff, kv, L + slot);
          kv += 12288;
          wait_vm<24>();
        } else {
          wait_vm<12>();
        }
        slot = next_slot(slot);
      }
      bar();
      const Tail<2> tl = tail_init<2>(L, w, l, wo_h0, b);
      {
        const unsigned lin[4] = {l * 16u, l * 16u, l * 16u, l * 16u};
#pragma unroll
        for (int g = 0; g < 10; ++g) {
          const int off = g < 6 ? (g / 3) * 245760 + b * 10240 + (g % 3) * 3072 : 3 * 245760 + b * 15360 + (g < 9 ? (g - 6) * 4096 : 11264);
          dma4<2 * SLOT + 16384>(lin, wo_h0 + off, L);
        }
      }
      wait_vm<44>();
      bar();
#pragma unroll
      for (int f = 0; f < 24; ++f) lds_w128(L + l * 16 + f * 1024, pk0[f]);
      bar();
#pragma unroll
      for (int f = 0; f < 24; ++f) lds_w128(L + SLOT + l * 16 + f * 1024, pk1[f]);
      tail_run<2, false>(tl, L, w, l, part, hg * 15, nullptr);
    }
  } else {
    const int a = w - 4, h = l >> 5, g = l >> 4, q = (l >> 2) & 3, pp = l & 3;
    if (w == 4 && l < 8) {
      expect = __hip_atomic_load(mail + hg * 64 + l, RLX_AGENT) + 1;
      __hip_atomic_store(mail + l * 64 + 32 + hg, expect, RLX_AGENT);
    }
    u32x4 xf[40];
    const unsigned xa = L + (a * 32 + (l & 31)) * 256, xsw = l & 15;
    unsigned wa[2];
    const int ps = ((pp & 1) << 1) | (pp >> 1);
#pragma unroll
    for (int e = 0; e < 2; ++e) wa[e] = L + OFF_W + (8 * h + q) * 128 + ((e ^ (q >> 1)) << 6) + (g & 1) * 32 + ps * 8;
    const unsigned qaddr = L + OFF_H + a * 1024 + l * 16;
    unsigned slot = 2 * SLOT;
    auto qthird = [&](int third, const bool first) __attribute__((always_inline)) {
        f32x16 acc[2];
#pragma unroll
        for (int i = 0; i < 16; ++i) { acc[0][i] = 0.f; acc[1][i] = 0.f; }
        bf16x8 wf[84];
#pragma unroll
        for (int kb = 0; kb < 5; ++kb) {
          bar();
          unsigned w0 = wa[0] + slot, w1 = wa[1] + slot;
          PIN(w0); PIN(w1);
          if (first) {
#pragma unroll
            for (int s = 0; s < 8; ++s) xf[kb * 8 + s] = lds_r128(xa + slot + (((2 * s + h) ^ xsw) << 4));
          }
          auto rd = [&](int r) __attribute__((always_inline)) {
            const unsigned ad = ((r & 1) ? w1 : w0) + (r >> 1) * 2048;
            wf[kb * 16 + r] = lds_tr2(ad, ad + 512);
          };
#pragma unroll
          for (int i = 0; i < 16; ++i) {
            const int c = kb * 16 + i - 4;
            if (i >= 4) acc[c & 1] = mma32(wf[c], asbf(xf[c >> 1]), acc[c & 1]);
            rd(i);
            if (i < 4 && c >= 0) acc[c & 1] = mma32(wf[c], asbf(xf[c >> 1]), acc[c & 1]);
            FENCE();
          }
          slot = next_slot(slot);
        }
#pragma unroll
        for (int c = 76; c < 80; ++c) acc[c & 1] = mma32(wf[c], asbf(xf[c >> 1]), acc[c & 1]);
#pragma unroll
        for (int f = 0; f < 4; ++f) lds_w128(qaddr + (third * 4 + f) * 3072, pk8(acc[f >> 1], (f & 1) * 8, 1.0f));
    };
    qthird(0, true);
#pragma unroll 1
    for (int i = 1; i < 12; ++i) {
      const int third = i % 3;
      qthird(third, false);
      if (third == 2) { bar(); slot = next_slot(slot); }
    }
    if (w == 4) sent = __hip_atomic_load(mail + hg * 64 + 32 + (l & 7), RLX_AGENT);
    const Tail<3> tl = tail_init<3>(L, w, l, wo_h0, b);
    tail_run<3, false>(tl, L, w, l, part, hg * 15, nullptr);
  }
  const int l8 = l & 7;
  wait_vm<0>();
  bar();
  auto refresh = [&]() __attribute__((always_inline)) {
    const unsigned r = __hip_atomic_load(mail + hg * 64 + 32 + l8, RLX_AGENT);
    if (r != sent && l < 8) __hip_atomic_store(mail + l * 64 + hg, r, RLX_AGENT);
    sent = r;
  };
  if (w == 4) {
    if (l < 8) __hip_atomic_store(mail + l * 64 + hg, sent, RLX_AGENT);
#pragma unroll 1
    for (int spin = 0; spin < (1 << 20); ++spin) {
      const unsigned m = __hip_atomic_load(mail + hg * 64 + l8, RLX_AGENT);
      if (__builtin_amdgcn_ballot_w64(l < 8 && m != expect) == 0) break;
      if ((spin & 15) == 15) refresh();
      __builtin_amdgcn_s_sleep(1);
    }
  }
  bar();
  const char* grp = args.part + (size_t)(br * 16 + b) * 8 * 122880 + l * 16;
  u32x4 v[2][8];
#pragma unroll
  for (int p = 0; p < 2; ++p) {
    const int k = p && w < 7 ? w + 8 : w;
    v[p][0] = lds_r128(L + own_off(k) + l * 16);
#pragma unroll
    for (int i = 1; i < 8; ++i)
      asm volatile("global_load_dwordx4 %0, %1, off sc0 sc1" : "=v"(v[p][i]) : "v"(grp + ((hg + i) & 7) * 122880 + (hg * 15 + k) * 1024) : "memory");
  }
  wait_vm<0>();
#pragma unroll
  for (int i = 0; i < 16; ++i) PIN(v[i >> 3][i & 7]);
#pragma unroll
  for (int p = 0; p < 2; ++p) {
    float s[8];
#pragma unroll
    for (int e = 0; e < 4; ++e) {
      s[2 * e] = 0.f; s[2 * e + 1] = 0.f;
#pragma unroll
      for (int i = 0; i < 8; ++i) { s[2 * e] += __uint_as_float(v[p][i][e] << 16); s[2 * e + 1] += __uint_as_float(v[p][i][e] & 0xffff0000u); }
    }
    const int pc = hg * 15 + w + 8 * p, nb = pc / 6, m = (pc % 6) >> 1, sh = pc & 1;
    char* dst = args.out + (size_t)(b * 96 + m * 32 + (l & 31)) * 2560 + (br * 640 + nb * 32 + sh * 16 + (l >> 5) * 8) * 2;
    if (p == 0 || w < 7) *(u32x4*)dst = u32x4{pk2(s[0], s[1]), pk2(s[2], s[3]), pk2(s[4], s[5]), pk2(s[6], s[7])};
  }
  if (w == 4) refresh();
}

extern "C" void kernel_launch(void* const* d_in, const int* in_sizes, int n_in, void* d_out, int out_size, void* d_ws,
                              size_t ws_size, hipStream_t stream) {
  Args a;
  for (int i = 0; i < 2; ++i) {
    a.x[i] = (const char*)d_in[5 * i]; a.k[i] = (const char*)d_in[5 * i + 1]; a.v[i] = (const char*)d_in[5 * i + 2];
    a.wq[i] = (const char*)d_in[5 * i + 3]; a.wo[i] = (const char*)d_in[5 * i + 4];
  }
  a.part = (char*)d_ws;
  a.out = (char*)d_out;
  fused_attn_kernel<<<256, 512, 0, stream>>>(a);
}
```

```cpp
#include <hip/hip_runtime.h>
#include <hip/hip_bf16.h>

typedef __attribute__((ext_vector_type(8))) __bf16 bf16x8;
typedef __attribute__((ext_vector_type(2))) __bf16 bf16x2;
typedef __attribute__((ext_vector_type(4))) short s16x4;
typedef __attribute__((ext_vector_type(8))) short s16x8;
typedef __attribute__((ext_vector_type(2))) float f32x2;
typedef __attribute__((ext_vector_type(16))) float f32x16;
typedef __attribute__((ext_vector_type(2))) unsigned u32x2;
typedef __attribute__((ext_vector_type(4))) unsigned u32x4;
#define LDS_AS __attribute__((address_space(3)))
#define DEV __device__ __forceinline__

constexpr int SLOT = 40960, OFF_V = 12288, OFF_W = 24576, OFF_H = 122880;
constexpr int OFF_DUMMY = 159744, LDS_BYTES = 163840;
constexpr float SM_C = 0.07216878364870322f * 1.4426950408889634f;
constexpr float SM_THR = 8.0f;

DEV void dma16(unsigned voff, const char* sbase, unsigned ldsdst) {
  asm volatile("s_mov_b32 m0, %2\n\ts_nop 0\n\tglobal_load_lds_dwordx4 %0, %1" ::"v"(voff), "s"(sbase), "s"(ldsdst) : "memory");
}
template <int D> DEV void dma4(const unsigned* v, const char* sbase, unsigned ldsdst) {
  asm volatile("s_add_u32 m0, %5, %6\n\ts_nop 0\n\tglobal_load_lds_dwordx4 %0, %4\n\tglobal_load_lds_dwordx4 %1, %4 offset:1024\n\t"
               "global_load_lds_dwordx4 %2, %4 offset:2048\n\tglobal_load_lds_dwordx4 %3, %4 offset:3072"
               ::"v"(v[0]), "v"(v[1]), "v"(v[2]), "v"(v[3]), "s"(sbase), "s"(ldsdst), "n"(D) : "memory", "scc");
}
template <int D> DEV void dma3nt(const unsigned* v, const char* sbase, unsigned ldsdst) {
  asm volatile("s_add_u32 m0, %4, %5\n\ts_nop 0\n\tglobal_load_lds_dwordx4 %0, %3 nt\n\tglobal_load_lds_dwordx4 %1, %3 offset:1024 nt\n\t"
               "global_load_lds_dwordx4 %2, %3 offset:2048 nt"
               ::"v"(v[0]), "v"(v[1]), "v"(v[2]), "s"(sbase), "s"(ldsdst), "n"(D) : "memory", "scc");
}
DEV void dma_kv(const unsigned* v, const char* src, unsigned dst) {
  dma3nt<0>(v, src, dst); dma3nt<3072>(v + 3, src, dst);
  dma3nt<6144>(v, src + 6144, dst); dma3nt<9216>(v + 3, src + 6144, dst);
}
template <int N> DEV void wait_vm() { asm volatile("s_waitcnt vmcnt(%0)" ::"n"(N) : "memory"); }
DEV void bar() { asm volatile("s_waitcnt lgkmcnt(0)\n\ts_barrier" ::: "memory"); }
DEV u32x4 lds_r128(unsigned a) { return *(LDS_AS const u32x4*)a; }
DEV void lds_w128(unsigned a, u32x4 v) { *(LDS_AS u32x4*)a = v; }
DEV bf16x8 lds_tr2(unsigned a0, unsigned a1) {
  s16x4 lo = __builtin_amdgcn_ds_read_tr16_b64_v4i16((LDS_AS s16x4*)a0);
  s16x4 hi = __builtin_amdgcn_ds_read_tr16_b64_v4i16((LDS_AS s16x4*)a1);
  s16x8 v = __builtin_shufflevector(lo, hi, 0, 1, 2, 3, 4, 5, 6, 7);
  return __builtin_bit_cast(bf16x8, v);
}
DEV unsigned pk2(float a, float b) {
  f32x2 f = {a, b};
  return __builtin_bit_cast(unsigned, __builtin_convertvector(f, bf16x2));
}
DEV u32x4 pk8(const f32x16& v, int o, float s) {
  u32x4 r;
#pragma unroll
  for (int i = 0; i < 4; ++i) {
    const f32x2 f = f32x2{v[o + 2 * i], v[o + 2 * i + 1]} * f32x2{s, s};
    r[i] = __builtin_bit_cast(unsigned, __builtin_convertvector(f, bf16x2));
  }
  return r;
}
DEV f32x16 mma32(bf16x8 a, bf16x8 b, f32x16 c) { return __builtin_amdgcn_mfma_f32_32x32x16_bf16(a, b, c, 0, 0, 0); }
DEV bf16x8 asbf(u32x4 v) { return __builtin_bit_cast(bf16x8, v); }
DEV float xhalf_max(float v) {
  u32x2 r = __builtin_amdgcn_permlane32_swap(__float_as_uint(v), __float_as_uint(v), false, false);
  return fmaxf(__uint_as_float(r.x), __uint_as_float(r.y));
}
DEV float xhalf_sum(float v) {
  u32x2 r = __builtin_amdgcn_permlane32_swap(__float_as_uint(v), __float_as_uint(v), false, false);
  return __uint_as_float(r.x) + __uint_as_float(r.y);
}
DEV unsigned next_slot(unsigned s) { return s == 2 * SLOT ? 0 : s + SLOT; }

#define FENCE() __builtin_amdgcn_sched_barrier(0)
#define PIN(x) asm volatile("" : "+v"(x))
DEV unsigned early_off(int e) { return e == 0 ? OFF_W + 12288 : e == 7 ? OFF_DUMMY : (e < 4 ? SLOT : 2 * SLOT - 9216) + OFF_W + 3072 + e * 3072; }
DEV void wo_warm(const char* wo_h0, int b, int i, int l, unsigned dst) {
  const int off = i < 10 ? (i < 5 ? 160 + i : 395 + i) * 1024 + b * 5120 : 2 * 245760 + b * 15360 + (i < 25 ? i - 10 : 0) * 1024;
  dma16(l * 16, wo_h0 + off, dst);
}
DEV unsigned own_off(unsigned k) { return (k < 9 ? SLOT : 2 * SLOT - 9216) + OFF_W + 6144 + k * 1024; }
template <int NB> struct Tail {
  unsigned voff[NB], aa[NB], wbuf, tabw, tabo;
  const char* wo;
  int nb0;
  DEV const char* wsrc(int n) const { return wo + (unsigned)__builtin_amdgcn_readlane(tabw, n); }
  DEV void dma(const char* src, int buf, int p) const { dma16(voff[p], src, wbuf + buf * (NB * 1024) + p * 1024); }
};
template <int NB> DEV Tail<NB> tail_init(unsigned L, int w, int l, const char* wo_h0, int b) {
  Tail<NB> t;
  constexpr int RB = NB * 64;
  const int pr = w & 3;
  t.nb0 = 5 * pr + ((pr & 1) ? (NB == 2 ? 3 : 0) : (NB == 2 ? 0 : 2));
  const int h = l >> 5, g = l >> 4, q = (l >> 2) & 3, ps = ((l & 1) << 1) | ((l >> 1) & 1);
  t.wbuf = L + (NB == 2 ? 2 * SLOT + pr * 4096 : OFF_W + (pr < 2 ? pr * 6144 : (pr - 1) * SLOT));
  t.wo = wo_h0;
#pragma unroll
  for (int p = 0; p < NB; ++p) {
    const int n = 64 * p + l, row = n / (4 * NB), c = n % (4 * NB);
    t.voff[p] = row * 1280 + t.nb0 * 64 + ((NB == 2 ? c ^ (((row >> 1) & 1) << 2) : c) << 4);
    t.aa[p] = t.wbuf + (4 * h + q) * RB + ((NB == 2 ? p ^ (q >> 1) : p) << 6) + (g & 1) * 32 + ps * 8;
  }
  const int n = l < 47 ? l : 47, e = (n + 4 + b) & 7, f12 = (n + b) % 12;
  int hd, f;
  if (n < 12 || n >= 36) { hd = n < 12 ? 2 : 3; f = f12; t.tabo = OFF_H + f * 3072; }
  else if (n < 20) { hd = e >> 2; f = 8 + (e & 3); t.tabo = early_off(e); }
  else { hd = n >= 28; f = e; t.tabo = hd * SLOT + f * 3072; }
  t.tabo += L;
  t.tabw = (hd * 192 + f * 16) * 1280;
#pragma unroll
  for (int p = 0; p < 2 * NB; ++p) t.dma(t.wsrc(p / NB), p / NB, p % NB);
  return t;
}
template <int NB, bool ATT>
DEV void tail_run(const Tail<NB>& t, unsigned L, int w, int l, char* part, int own0, const u32x4* obf) {
  constexpr int RB = NB * 64;
  auto oadr = [&](int n) __attribute__((always_inline)) {
    unsigned a = (unsigned)__builtin_amdgcn_readlane(t.tabo, n) + l * 16;
    PIN(a);
    return a;
  };
  f32x16 acc[NB][3];
  const f32x16 zero = {0.f, 0.f, 0.f, 0.f, 0.f, 0.f, 0.f, 0.f, 0.f, 0.f, 0.f, 0.f, 0.f, 0.f, 0.f, 0.f};
  bf16x8 af[2][NB], bf[2][3];
  if (NB == 3) { bar(); bar(); }
  wait_vm<NB>();
  {
    const unsigned oa = oadr(0);
#pragma unroll
    for (int j = 0; j < NB; ++j) af[0][j] = lds_tr2(t.aa[j], t.aa[j] + 8 * RB);
#pragma unroll
    for (int m = 0; m < 3; ++m) bf[0][m] = asbf(lds_r128(oa + m * 1024));
    asm volatile("s_waitcnt lgkmcnt(0)" ::: "memory");
#pragma unroll
    for (int p = 0; p < NB; ++p) t.dma(t.wsrc(2), 0, p);
  }
  auto step = [&](int n, const int c, const bool first) __attribute__((always_inline)) {
    unsigned oa;
    const char* src;
    wait_vm<NB>();
    FENCE();
#pragma unroll
    for (int k = 0; k < 3 * NB; ++k) {
      const int j = k / 3, m = k % 3;
      acc[j][m] = mma32(af[c][j], bf[c][m], first ? zero : acc[j][m]);
      auto rda = [&](int j2) __attribute__((always_inline)) {
        const unsigned ad = t.aa[j2] + (c ^ 1) * (NB * 1024);
        af[c ^ 1][j2] = lds_tr2(ad, ad + 8 * RB);
      };
      if (k == 0) { rda(0); rda(1); }
      if (k == 1 && NB == 3) rda(2);
      if (k == NB - 1) oa = oadr(n + 1);
      if (k == 2 * NB - 1) { src = t.wsrc(n + 3); asm volatile("" : "+s"(src)); }
      if (k >= NB - 1 && k < NB + 2) bf[c ^ 1][k - NB + 1] = asbf(lds_r128(oa + (k - NB + 1) * 1024));
      if (k == 2 * NB) asm volatile("s_waitcnt lgkmcnt(3)" ::: "memory");
      if (k >= 2 * NB) t.dma(src, c ^ 1, k - 2 * NB);
      FENCE();
    }
  };
  constexpr unsigned long long bm = NB == 2 ? (1ull << 10) | (1ull << 22) : 0x5f7dfull | (1ull << 26) | (1ull << 32);
  step(0, 0, true);
  if (ATT) __builtin_amdgcn_s_setprio(0);
  if (NB == 3) bar();
  step(1, 1, false);
  if (NB == 3) bar();
#pragma unroll 1
  for (int n = 2; n < 48; n += 2) {
    step(n, 0, false);
    if ((bm >> n) & 1) {
      bar();
      if (ATT && n == 10)
#pragma unroll
      for (int f = 0; f < 12; ++f) lds_w128(L + OFF_H + w * 1024 + l * 16 + f * 3072, obf[f]);
    }
    step(n + 1, 1, false);
    if (NB == 3 && ((bm >> n) & 2)) bar();
  }
  wait_vm<0>();
  char* dst = part + t.nb0 * 6144 + l * 16;
#pragma unroll
  for (int j = 0; j < NB; ++j)
#pragma unroll
    for (int m = 0; m < 3; ++m)
#pragma unroll
      for (int s = 0; s < 2; ++s) {
        const int i = (j * 3 + m) * 2 + s;
        const unsigned k = t.nb0 * 6 + i - own0;
        const u32x4 v = pk8(acc[j][m], s * 8, 1.0f);
        if (k < 15) lds_w128(L + own_off(k) + l * 16, v);
        else asm volatile("global_store_dwordx4 %0, %1, off sc0 sc1" ::"v"(dst + i * 1024), "v"(v) : "memory");
      }
}

struct Att {
  f32x16 o[6];
  u32x4 q[12];
  float z[16], mc, lsum;
  unsigned ka[4], va[2];
};
struct Adr { unsigned k[4], v[2]; };
DEV Adr att_adr(const Att& a, unsigned slot) {
  Adr d;
#pragma unroll
  for (int i = 0; i < 4; ++i) { d.k[i] = a.ka[i] + slot; PIN(d.k[i]); }
#pragma unroll
  for (int i = 0; i < 2; ++i) { d.v[i] = a.va[i] + slot; PIN(d.v[i]); }
  return d;
}
DEV bf16x8 rd_k(const Adr& d, int k) { return asbf(lds_r128(d.k[k & 3] + (k >> 2) * 128)); }
DEV bf16x8 rd_v(const Adr& d, int i) {
  const unsigned ad = d.v[i & 1] + ((i % 6) >> 1) * 128 + (i / 6) * 16 * 384;
  return lds_tr2(ad, ad + 8 * 384);
}
DEV float max3(float a, float b, float c) { return fmaxf(fmaxf(a, b), c); }
template <bool QK, bool PV, bool FIRST = false> DEV float att_beat(Att& a, unsigned slot) {
  const Adr d = att_adr(a, slot);
  const f32x16 zero = {0.f, 0.f, 0.f, 0.f, 0.f, 0.f, 0.f, 0.f, 0.f, 0.f, 0.f, 0.f, 0.f, 0.f, 0.f, 0.f};
  f32x16 s = zero;
  bf16x8 kf[12], vf[12];
  unsigned pn[8];
  float t0 = a.lsum, t1 = 0.f;
  auto ex = [&](int i) __attribute__((always_inline)) {
    const float e0 = __builtin_amdgcn_exp2f(a.z[2 * i]), e1 = __builtin_amdgcn_exp2f(a.z[2 * i + 1]);
    t0 += e0; PIN(t0); t1 += e1; PIN(t1);
    pn[i] = pk2(e0, e1); PIN(pn[i]);
  };
  if (QK) {
#pragma unroll
    for (int k = 0; k < 4; ++k) kf[k] = rd_k(d, k);
  }
  FENCE();
  if (PV) { ex(0); ex(1); }
  FENCE();
#pragma unroll
  for (int k = 0; k < 12; ++k) {
    if (QK) s = mma32(kf[k], asbf(a.q[k]), s);
    if (k < 8) { if (QK) kf[k + 4] = rd_k(d, k + 4); }
    else if (PV) vf[k - 8] = rd_v(d, k - 8);
    if (PV && k < 6) ex(k + 2);
    FENCE();
  }
  if (PV) a.lsum = t0 + t1;
  const bf16x8 p0 = asbf(u32x4{pn[0], pn[1], pn[2], pn[3]}), p1 = asbf(u32x4{pn[4], pn[5], pn[6], pn[7]});
  float mx = -3.0e38f;
#pragma unroll
  for (int i = 0; i < 12; ++i) {
    if (PV) {
      a.o[i % 6] = mma32(vf[i], i < 6 ? p0 : p1, FIRST && i < 6 ? zero : a.o[i % 6]);
      if (i < 8) vf[i + 4] = rd_v(d, i + 4);
    }
    if (QK && i >= 3 && i < 11) {
      const int j = 2 * (i - 3);
      float z0 = __builtin_fmaf(s[j], SM_C, -a.mc), z1 = __builtin_fmaf(s[j + 1], SM_C, -a.mc);
      mx = max3(mx, z0, z1);
      PIN(z0); PIN(z1); PIN(mx);
      a.z[j] = z0; a.z[j + 1] = z1;
    }
    FENCE();
  }
  return mx;
}
DEV void att_shift(Att& a, float dl) {
  a.mc += dl;
#pragma unroll
  for (int i = 0; i < 16; ++i) a.z[i] -= dl;
}

struct Args { const char *x[2], *k[2], *v[2], *wq[2], *wo[2]; char *part, *out; };
typedef __attribute__((address_space(1))) unsigned gu32;
#define RLX_AGENT __ATOMIC_RELAXED, __HIP_MEMORY_SCOPE_AGENT

__global__ __launch_bounds__(512, 2) void fused_attn_kernel(Args args) {
  __shared__ __attribute__((aligned(1024))) char lds[LDS_BYTES];
  const unsigned L = (unsigned)(size_t)(LDS_AS char*)lds;
  const int w = __builtin_amdgcn_readfirstlane(threadIdx.x >> 6), l = threadIdx.x & 63;
  const int bid = blockIdx.x, xcd = bid & 7, ii = bid >> 3;
  const int br = xcd >> 2, b = ii & 15, hg = (xcd & 3) * 2 + (ii >> 4), h0 = hg * 4;
  const char* wo_h0 = (br ? args.wo[1] : args.wo[0]) + (size_t)h0 * 192 * 1280;
  char* part = args.part + (size_t)((br * 16 + b) * 8 + hg) * 122880;

  gu32* mail = (gu32*)(args.part + 256 * 122880 + (br * 16 + b) * 2048);
  unsigned expect = 0, sent = 0;

  if (w < 3) {
    Att a;
    __builtin_amdgcn_s_setprio(1);
    const int r = l & 31, h = l >> 5, g = l >> 4, q = (l >> 2) & 3, pp = l & 3;
#pragma unroll
    for (int s = 0; s < 4; ++s) a.ka[s] = L + r * 384 + (((2 * s + h) ^ ((r >> 1) & 7)) << 4);
#pragma unroll
    for (int e = 0; e < 2; ++e) a.va[e] = L + OFF_V + (4 * h + q) * 384 + ((e ^ (q >> 1)) << 6) + (g & 1) * 32 + pp * 8;
    const unsigned qaddr = L + OFF_H + w * 1024 + l * 16;
    {
      const char* src = (br ? args.wq[1] : args.wq[0]) + h0 * 384 + w * 128 + (size_t)b * 8 * 12288;
      const unsigned off = (l >> 3) * 12288 + (l & 7) * 16;
#pragma unroll
      for (int kb = 0; kb < 5; ++kb) dma16(off, src + (size_t)kb * 128 * 12288, L + OFF_DUMMY + w * 1024);
    }
#pragma unroll 1
    for (int t = 0; t < 16; ++t) {
      bar();
      if (t >= 7) wo_warm(wo_h0, b, 3 * (t - 7) + w, l, L + OFF_DUMMY + w * 1024);
      if (t >= 1) {
        const int hh = (t - 1) / 5, kb = (t - 1) % 5;
        const char* src = (br ? args.wq[1] : args.wq[0]) + (h0 + 1 + hh) * 384 + w * 128 + (size_t)(kb * 128 + b * 8) * 12288;
        dma16((l >> 3) * 12288 + (l & 7) * 16, src, L + OFF_DUMMY + w * 1024);
      }
    }
#pragma unroll
    for (int f = 0; f < 12; ++f) a.q[f] = lds_r128(qaddr + f * 3072);
    unsigned slot = 0;
    a.lsum = 0.f;
#pragma unroll 1
    for (int rd = 0; rd < 4; ++rd) {
      {
        bar();
        a.mc = 0.f;
        float mx;
        if (rd > 0) {
          mx = att_beat<true, true>(a, slot);
          const float inv = 1.0f / xhalf_sum(a.lsum);
#pragma unroll
          for (int f = 0; f < 12; ++f) lds_w128(qaddr + f * 3072, pk8(a.o[f >> 1], (f & 1) * 8, inv));
        } else {
          mx = att_beat<true, false>(a, slot);
        }
        a.lsum = 0.f;
        att_shift(a, xhalf_max(mx));
        slot = next_slot(slot);
      }
      auto rescale = [&](float mx) __attribute__((always_inline)) {
        if (__builtin_amdgcn_ballot_w64(mx > SM_THR) != 0) {
          const float dl = fmaxf(xhalf_max(mx), 0.f), alpha = __builtin_amdgcn_exp2f(-dl);
          att_shift(a, dl);
          a.lsum *= alpha;
#pragma unroll
          for (int bb = 0; bb < 6; ++bb) a.o[bb] *= alpha;
        }
        slot = next_slot(slot);
      };
      bar();
      rescale(att_beat<true, true, true>(a, slot));
#pragma unroll 1
      for (int u = 2; u < 16; ++u) {
        bar();
        rescale(att_beat<true, true>(a, slot));
      }
      if (rd < 3) {
        wait_vm<0>();
#pragma unroll
        for (int f = 0; f < 12; ++f) a.q[f] = lds_r128(qaddr + f * 3072);
      }
    }
    const Tail<2> tl = tail_init<2>(L, w, l, wo_h0, b);
    bar();
    att_beat<false, true>(a, slot);
    bar();
    const float inv = 1.0f / xhalf_sum(a.lsum);
    u32x4 obf[12];
#pragma unroll
    for (int f = 0; f < 12; ++f) obf[f] = pk8(a.o[f >> 1], (f & 1) * 8, inv);
    tail_run<2, true>(tl, L, w, l, part, hg * 15, obf);
  } else if (w == 3 || w == 7) {
    const int isv = w >> 2;
    const char* kv = (isv ? (br ? args.v[1] : args.v[0]) : (br ? args.k[1] : args.k[0])) + (size_t)(b * 32 + h0) * 512 * 384;
    const char* wq = (br ? args.wq[1] : args.wq[0]) + h0 * 384 + (size_t)isv * 64 * 12288;
    unsigned koff[6];
#pragma unroll
    for (int p = 0; p < 6; ++p) {
      const int n = 64 * p + l, row = n / 24, c = n % 24;
      koff[p] = row * 384 + ((c ^ (isv ? ((row >> 1) & 1) << 2 : (row >> 1) & 7)) << 4) - (p % 3) * 1024;
    }
    unsigned woff[4], xoff[4];
#pragma unroll
    for (int p = 0; p < 4; ++p) {
      const int row = 4 * p + (l >> 4);
      xoff[p] = row * 1280 + (((l & 15) ^ row) << 4) - p * 1024;
      woff[p] = (8 * p + (l >> 3)) * 12288 + (((l & 7) ^ (((l >> 4) & 1) << 2)) << 4) - p * 1024;
    }
    const char* xs = (br ? args.x[1] : args.x[0]) + (size_t)(b * 96 + isv * 48) * 1280;
    unsigned slot = 2 * SLOT;
    int kb = 0;
    auto wtile = [&]() __attribute__((always_inline)) {
      const unsigned dst = L + slot + isv * 8192;
      dma4<OFF_W>(woff, wq, dst);
      dma4<OFF_W + 4096>(woff, wq + 32 * 12288, dst);
      wq += kb == 4 ? 128 - 4 * 128 * 12288 : 128 * 12288;
      kb = kb == 4 ? 0 : kb + 1;
    };
#pragma unroll 1
    for (int t = -16; t < -11; ++t) {
      if (t > -15) bar();
      const unsigned dst = L + slot + isv * OFF_V;
      dma4<0>(xoff, xs, dst); dma4<4096>(xoff, xs + 16 * 1280, dst); dma4<8192>(xoff, xs + 32 * 1280, dst);
      xs += 256;
      wtile();
      wait_vm<20>();
      slot = next_slot(slot);
    }
    unsigned long long mw = 0x03fffbfffbfffbffull, mk = ~0x3fffull | (isv ? 0x18 : 0x1c);
    auto beat = [&]() __attribute__((always_inline)) {
      bar();
      if (mw & 1) wtile();
      if (mk & 1) {
        dma_kv(koff, kv, L + slot + isv * OFF_V);
        kv += 12288;
        if (mw & 1) wait_vm<20>(); else wait_vm<12>();
      } else {
        if (mw & 1) wait_vm<8>(); else wait_vm<0>();
      }
      mw >>= 1; mk >>= 1;
      slot = next_slot(slot);
    };
#pragma unroll 1
    for (int t = -13; t <= 17; ++t) beat();
    if (isv) {
      u32x4 pk[24];
#pragma unroll
      for (int f = 0; f < 12; ++f) pk[f] = lds_r128(L + OFF_H + 24576 + l * 16 + f * 1024);
#pragma unroll 1
      for (int t = 18; t <= 33; ++t) beat();
#pragma unroll
      for (int f = 0; f < 12; ++f) pk[12 + f] = lds_r128(L + OFF_H + 24576 + l * 16 + f * 1024);
#pragma unroll 1
      for (int t = 34; t <= 46; ++t) beat();
      bar();
#pragma unroll
      for (int f = 0; f < 24; ++f) lds_w128(L + early_off(f / 3) + (f % 3) * 1024 + l * 16, pk[f]);
      const Tail<3> tl = tail_init<3>(L, w, l, wo_h0, b);
      wait_vm<6>();
      tail_run<3, false>(tl, L, w, l, part, hg * 15, nullptr);
    } else {
      u32x4 pk0[24], pk1[24];
#pragma unroll
      for (int f = 0; f < 24; ++f) pk0[f] = lds_r128(L + OFF_H + l * 16 + f * 1024);
#pragma unroll 1
      for (int t = 18; t <= 33; ++t) beat();
#pragma unroll
      for (int f = 0; f < 24; ++f) pk1[f] = lds_r128(L + OFF_H + l * 16 + f * 1024);
#pragma unroll 1
      for (int t = 34; t <= 46; ++t) beat();
      const char* vv = (br ? args.v[1] : args.v[0]) + (size_t)(b * 32 + h0) * 512 * 384 + 48 * 12288;
      unsigned voff[6];
#pragma unroll
      for (int p = 0; p < 6; ++p) {
        const int n = 64 * p + l, row = n / 24, c = n % 24;
        voff[p] = row * 384 + ((c ^ (((row >> 1) & 1) << 2)) << 4) - (p % 3) * 1024;
      }
#pragma unroll 1
      for (int t = 49; t <= 64; ++t) {
        bar();
        dma_kv(voff, vv, L + slot + OFF_V);
        vv += 12288;
        if (t < 64) {
          dma_kv(koff, kv, L + slot);
          kv += 12288;
          wait_vm<24>();
        } else {
          wait_vm<12>();
        }
        slot = next_slot(slot);
      }
      bar();
      const Tail<2> tl = tail_init<2>(L, w, l, wo_h0, b);
      {
        const unsigned lin[4] = {l * 16u, l * 16u, l * 16u, l * 16u};
#pragma unroll
        for (int g = 0; g < 10; ++g) {
          const int off = g < 6 ? (g / 3) * 245760 + b * 10240 + (g % 3) * 3072 : 3 * 245760 + b * 15360 + (g < 9 ? (g - 6) * 4096 : 11264);
          dma4<2 * SLOT + 16384>(lin, wo_h0 + off, L);
        }
      }
      wait_vm<44>();
      bar();
#pragma unroll
      for (int f = 0; f < 24; ++f) lds_w128(L + l * 16 + f * 1024, pk0[f]);
      bar();
#pragma unroll
      for (int f = 0; f < 24; ++f) lds_w128(L + SLOT + l * 16 + f * 1024, pk1[f]);
      tail_run<2, false>(tl, L, w, l, part, hg * 15, nullptr);
    }
  } else {
    const int a = w - 4, h = l >> 5, g = l >> 4, q = (l >> 2) & 3, pp = l & 3;
    if (w == 4 && l < 8) {
      expect = __hip_atomic_load(mail + hg * 64 + l, RLX_AGENT) + 1;
      __hip_atomic_store(mail + l * 64 + 32 + hg, expect, RLX_AGENT);
    }
    u32x4 xf[40];
    const unsigned xa = L + (a * 32 + (l & 31)) * 256, xsw = l & 15;
    unsigned wa[2];
    const int ps = ((pp & 1) << 1) | (pp >> 1);
#pragma unroll
    for (int e = 0; e < 2; ++e) wa[e] = L + OFF_W + (8 * h + q) * 128 + ((e ^ (q >> 1)) << 6) + (g & 1) * 32 + ps * 8;
    const unsigned qaddr = L + OFF_H + a * 1024 + l * 16;
    unsigned slot = 2 * SLOT;
    auto qthird = [&](int third, const bool first) __attribute__((always_inline)) {
        f32x16 acc[2];
#pragma unroll
        for (int i = 0; i < 16; ++i) { acc[0][i] = 0.f; acc[1][i] = 0.f; }
        bf16x8 wf[84];
#pragma unroll
        for (int kb = 0; kb < 5; ++kb) {
          bar();
          unsigned w0 = wa[0] + slot, w1 = wa[1] + slot;
          PIN(w0); PIN(w1);
          if (first) {
#pragma unroll
            for (int s = 0; s < 8; ++s) xf[kb * 8 + s] = lds_r128(xa + slot + (((2 * s + h) ^ xsw) << 4));
          }
          auto rd = [&](int r) __attribute__((always_inline)) {
            const unsigned ad = ((r & 1) ? w1 : w0) + (r >> 1) * 2048;
            wf[kb * 16 + r] = lds_tr2(ad, ad + 512);
          };
#pragma unroll
          for (int i = 0; i < 16; ++i) {
            const int c = kb * 16 + i - 4;
            if (i >= 4) acc[c & 1] = mma32(wf[c], asbf(xf[c >> 1]), acc[c & 1]);
            rd(i);
            if (i < 4 && c >= 0) acc[c & 1] = mma32(wf[c], asbf(xf[c >> 1]), acc[c & 1]);
            FENCE();
          }
          slot = next_slot(slot);
        }
#pragma unroll
        for (int c = 76; c < 80; ++c) acc[c & 1] = mma32(wf[c], asbf(xf[c >> 1]), acc[c & 1]);
#pragma unroll
        for (int f = 0; f < 4; ++f) lds_w128(qaddr + (third * 4 + f) * 3072, pk8(acc[f >> 1], (f & 1) * 8, 1.0f));
    };
    qthird(0, true);
#pragma unroll 1
    for (int i = 1; i < 12; ++i) {
      const int third = i % 3;
      qthird(third, false);
      if (third == 2) { bar(); slot = next_slot(slot); }
    }
    if (w == 4) sent = __hip_atomic_load(mail + hg * 64 + 32 + (l & 7), RLX_AGENT);
    const Tail<3> tl = tail_init<3>(L, w, l, wo_h0, b);
    tail_run<3, false>(tl, L, w, l, part, hg * 15, nullptr);
  }
  const int l8 = l & 7;
  wait_vm<0>();
  bar();
  auto refresh = [&]() __attribute__((always_inline)) {
    const unsigned r = __hip_atomic_load(mail + hg * 64 + 32 + l8, RLX_AGENT);
    if (r != sent && l < 8) __hip_atomic_store(mail + l * 64 + hg, r, RLX_AGENT);
    sent = r;
  };
  if (w == 4) {
    if (l < 8) __hip_atomic_store(mail + l * 64 + hg, sent, RLX_AGENT);
#pragma unroll 1
    for (int spin = 0; spin < (1 << 20); ++spin) {
      const unsigned m = __hip_atomic_load(mail + hg * 64 + l8, RLX_AGENT);
      if (__builtin_amdgcn_ballot_w64(l < 8 && m != expect) == 0) break;
      if ((spin & 15) == 15) refresh();
      __builtin_amdgcn_s_sleep(1);
    }
  }
  bar();
  const char* grp = args.part + (size_t)(br * 16 + b) * 8 * 122880 + l * 16;
  u32x4 v[2][8];
#pragma unroll
  for (int p = 0; p < 2; ++p) {
    const int k = p && w < 7 ? w + 8 : w;
    v[p][0] = lds_r128(L + own_off(k) + l * 16);
#pragma unroll
    for (int i = 1; i < 8; ++i)
      asm volatile("global_load_dwordx4 %0, %1, off sc0 sc1" : "=v"(v[p][i]) : "v"(grp + ((hg + i) & 7) * 122880 + (hg * 15 + k) * 1024) : "memory");
  }
  wait_vm<0>();
#pragma unroll
  for (int i = 0; i < 16; ++i) PIN(v[i >> 3][i & 7]);
#pragma unroll
  for (int p = 0; p < 2; ++p) {
    float s[8];
#pragma unroll
    for (int e = 0; e < 4; ++e) {
      s[2 * e] = 0.f; s[2 * e + 1] = 0.f;
#pragma unroll
      for (int i = 0; i < 8; ++i) { s[2 * e] += __uint_as_float(v[p][i][e] << 16); s[2 * e + 1] += __uint_as_float(v[p][i][e] & 0xffff0000u); }
    }
    const int pc = hg * 15 + w + 8 * p, nb = pc / 6, m = (pc % 6) >> 1, sh = pc & 1;
    char* dst = args.out + (size_t)(b * 96 + m * 32 + (l & 31)) * 2560 + (br * 640 + nb * 32 + sh * 16 + (l >> 5) * 8) * 2;
    if (p == 0 || w < 7) *(u32x4*)dst = u32x4{pk2(s[0], s[1]), pk2(s[2], s[3]), pk2(s[4], s[5]), pk2(s[6], s[7])};
  }
  if (w == 4) refresh();
}

extern "C" void kernel_launch(void* const* d_in, const int* in_sizes, int n_in, void* d_out, int out_size, void* d_ws,
                              size_t ws_size, hipStream_t stream) {
  Args a;
  for (int i = 0; i < 2; ++i) {
    a.x[i] = (const char*)d_in[5 * i]; a.k[i] = (const char*)d_in[5 * i + 1]; a.v[i] = (const char*)d_in[5 * i + 2];
    a.wq[i] = (const char*)d_in[5 * i + 3]; a.wo[i] = (const char*)d_in[5 * i + 4];
  }
  a.part = (char*)d_ws;
  a.out = (char*)d_out;
  fused_attn_kernel<<<256, 512, 0, stream>>>(a);
}
```
